```python
import torch

def _softmax(x, axis=-1):
    return torch.softmax(x, dim=axis)
def _gelu(x):
    return torch.nn.functional.gelu(x, approximate="tanh")
def _silu(x):
    return torch.nn.functional.silu(x)
def _sigmoid(x):
    return torch.sigmoid(x)
def _layernorm(x, axis=-1, eps=1e-5):
    m = x.mean(dim=axis, keepdim=True)
    v = x.var(dim=axis, keepdim=True, unbiased=False)
    return (x - m) / torch.sqrt(v + eps)
def _scatter_add(tgt, idx, src, axis=0):
    return tgt.clone().index_add_(axis, idx, src)
def _segment_sum(src, seg, n):
    out = torch.zeros((n,) + src.shape[1:], dtype=src.dtype)
    return out.index_add_(0, seg, src)
def _pad(x, pw):
    flat = tuple(v for pair in reversed(pw) for v in pair)
    return torch.nn.functional.pad(x, flat)

kernel_name = 'parallel_sum__mha_block__dense_activation_squared_relu__prompt'

def setup_inputs():

    _w = (lambda a: (lambda t: t.to(torch.bfloat16) if t.is_floating_point() else t)(torch.as_tensor(a)))
    def _A_setup():
        B, S, D, H = 1, 2048, 448, 16
        return {
            "x": _w((lambda *s: torch.randn(*s, dtype=torch.bfloat16) * 0.05)(B, S, D)),
            "wqkv": _w((lambda *s: torch.randn(*s, dtype=torch.bfloat16) * 0.2)(D, 3 * D)),
            "wo": _w((lambda *s: torch.randn(*s, dtype=torch.bfloat16) * 0.1)(D, D)),
        }
    _A_in = _A_setup()

    def _B_setup():
        B, S, D, F = 1, 2048, 448, 1792
        return {
            "x": _w((lambda *s: torch.randn(*s, dtype=torch.bfloat16) * 0.05)(B, S, D)),
            "w1": _w((lambda *s: torch.randn(*s, dtype=torch.bfloat16) * 0.1)(D, F)),
            "b1": _w((lambda *s: torch.randn(*s, dtype=torch.bfloat16) * 0.8)(F)),
            "w2": _w((lambda *s: torch.randn(*s, dtype=torch.bfloat16) * 0.1)(F, D)),
            "b2": _w((lambda *s: torch.randn(*s, dtype=torch.bfloat16) * 12.8)(D)),
        }
    _B_in = _B_setup()

    return {
        "A_x": _A_in["x"],
        "A_wqkv": _A_in["wqkv"],
        "A_wo": _A_in["wo"],
        "B_x": _B_in["x"],
        "B_w1": _B_in["w1"],
        "B_b1": _B_in["b1"],
        "B_w2": _B_in["w2"],
        "B_b2": _B_in["b2"],
    }

def reference(A_x, A_wqkv, A_wo, B_x, B_w1, B_b1, B_w2, B_b2):
    def _A_ref(x, wqkv, wo):
        B, S, D = x.shape
        H = 16
        Dh = D // H
        qkv = (lambda x, *d: x.permute(*d))((x @ wqkv).reshape(B, S, 3, H, Dh), 2, 0, 3, 1, 4)
        q, k, v = qkv[0], qkv[1], qkv[2]
        scores = torch.einsum("bhqd,bhkd->bhqk", q, k) * (Dh ** -0.5)
        p = _softmax(scores, axis=-1)
        o = torch.einsum("bhqk,bhkd->bhqd", p, v)
        return (lambda x, *d: x.permute(*d))(o, 0, 2, 1, 3).reshape(B, S, D) @ wo

    def _B_ref(x, w1, b1, w2, b2):
        h = torch.einsum("bsd,df->bsf", x, w1) + b1
        zero = torch.zeros_like(h)
        r = torch.where(h > zero, h, zero)
        h = r * r
        out = torch.einsum("bsf,fd->bsd", h, w2) + b2
        return out

    x = A_x
    xn = _layernorm(x, axis=-1)
    return x + _A_ref(xn, A_wqkv, A_wo) + _B_ref(xn, B_w1, B_b1, B_w2, B_b2)
```

```cpp
#include <hip/hip_runtime.h>
#include <hip/hip_bf16.h>
typedef __hip_bfloat16 bf16;
constexpr int S = 2048, D = 448, H = 16, DH = 28, F = 1792;
__device__ inline float b2f(bf16 x) { return __bfloat162float(x); }
__global__ void k_ln(const bf16* x, bf16* xn) {
    int r = blockIdx.x; int t = threadIdx.x;
    float v[7]; float s = 0;
    for (int i = 0; i < 7; i++) { v[i] = b2f(x[r * D + t + 64 * i]); s += v[i]; }
    for (int o = 32; o; o >>= 1) s += __shfl_xor(s, o);
    float m = s / D; float q = 0;
    for (int i = 0; i < 7; i++) { q += (v[i] - m) * (v[i] - m); }
    for (int o = 32; o; o >>= 1) q += __shfl_xor(q, o);
    float rs = rsqrtf(q / D + 1e-5f);
    for (int i = 0; i < 7; i++) xn[r * D + t + 64 * i] = __float2bfloat16((v[i] - m) * rs);
}
__global__ void k_gemm(const bf16* A, const bf16* B, const bf16* bias, bf16* C, int N, int K, int mode) {
    int n = blockIdx.x * 64 + threadIdx.x; int m = blockIdx.y;
    float acc = 0;
    for (int k = 0; k < K; k++) acc += b2f(A[m * K + k]) * b2f(B[k * N + n]);
    if (mode == 1) { acc += b2f(bias[n]); acc = acc > 0 ? acc * acc : 0.f; }
    C[m * N + n] = __float2bfloat16(acc);
}
__global__ void k_attn(const bf16* qkv, bf16* o) {
    int h = blockIdx.y; int q = blockIdx.x * 64 + threadIdx.x;
    float qv[DH]; for (int d = 0; d < DH; d++) qv[d] = b2f(qkv[q * 3 * D + h * DH + d]) * 0.1889822365046136f;
    float m = -1e30f, l = 0; float acc[DH]; for (int d = 0; d < DH; d++) acc[d] = 0;
    for (int k = 0; k < S; k++) {
        float s = 0; for (int d = 0; d < DH; d++) s += qv[d] * b2f(qkv[k * 3 * D + D + h * DH + d]);
        float mn = fmaxf(m, s); float a = __expf(m - mn); float p = __expf(s - mn); m = mn; l = l * a + p;
        for (int d = 0; d < DH; d++) acc[d] = acc[d] * a + p * b2f(qkv[k * 3 * D + 2 * D + h * DH + d]);
    }
    for (int d = 0; d < DH; d++) o[q * D + h * DH + d] = __float2bfloat16(acc[d] / l);
}
__global__ void k_fin(const bf16* x, const bf16* o, const bf16* wo, const bf16* hh, const bf16* w2, const bf16* b2, bf16* out) {
    int n = blockIdx.x * 64 + threadIdx.x; int m = blockIdx.y;
    float a = 0, b = 0;
    for (int k = 0; k < D; k++) a += b2f(o[m * D + k]) * b2f(wo[k * D + n]);
    for (int k = 0; k < F; k++) b += b2f(hh[m * F + k]) * b2f(w2[k * D + n]);
    out[m * D + n] = __float2bfloat16(b2f(x[m * D + n]) + a + b + b2f(b2[n]));
}
extern "C" void kernel_launch(void* const* d_in, const int* in_sizes, int n_in,
                              void* d_out, int out_size, void* d_ws, size_t ws_size,
                              hipStream_t stream) {
    const bf16* x = (const bf16*)d_in[0]; const bf16* wqkv = (const bf16*)d_in[1]; const bf16* wo = (const bf16*)d_in[2];
    const bf16* w1 = (const bf16*)d_in[4]; const bf16* b1 = (const bf16*)d_in[5]; const bf16* w2 = (const bf16*)d_in[6]; const bf16* b2 = (const bf16*)d_in[7];
    bf16* xn = (bf16*)d_ws; bf16* qkv = xn + S * D; bf16* hh = qkv + S * 3 * D; bf16* o = hh + S * F;
    k_ln<<<S, 64, 0, stream>>>(x, xn);
    k_gemm<<<dim3(3 * D / 64, S), 64, 0, stream>>>(xn, wqkv, nullptr, qkv, 3 * D, D, 0);
    k_gemm<<<dim3(F / 64, S), 64, 0, stream>>>(xn, w1, b1, hh, F, D, 1);
    k_attn<<<dim3(S / 64, H), 64, 0, stream>>>(qkv, o);
    k_fin<<<dim3(D / 64, S), 64, 0, stream>>>(x, o, wo, hh, w2, b2, (bf16*)d_out);
}
```

```cpp
#include <hip/hip_runtime.h>
#include <hip/hip_bf16.h>

struct Srd { const void* p; unsigned bytes; unsigned flags; };
struct Args { Srd s[9]; };

__global__ void __launch_bounds__(512, 2) fused_block_kernel(Args a) {
    __shared__ __attribute__((aligned(16))) char lds[163840];
    asm volatile(
        "s_load_dwordx16 s[32:47], %0, 0x0\n"
        "s_load_dwordx4 s[64:67], %0, 0x80\n"
        "s_load_dwordx16 s[48:63], %0, 0x40\n"
        "v_lshrrev_b32 v1, 6, v0\n"
        "s_and_b32 s13, %1, 7\n"
        "s_lshr_b32 s12, %1, 3\n"
        "v_readfirstlane_b32 s28, v1\n"
        "v_and_b32 v0, 63, v0\n"
        "s_and_b32 s18, s13, 3\n"
        "s_mul_i32 s18, s18, 4\n"
        "s_and_b32 s20, s12, 3\n"
        "s_add_u32 s8, s18, s20\n"
        "s_lshr_b32 s18, s13, 2\n"
        "s_mul_i32 s18, s18, 8\n"
        "s_lshr_b32 s19, s12, 2\n"
        "s_add_u32 s9, s18, s19\n"
        "s_mul_i32 s18, s20, 2\n"
        "s_add_u32 s18, s18, s9\n"
        "s_mul_i32 s19, s18, 37\n"
        "s_lshr_b32 s19, s19, 8\n"
        "s_mul_i32 s19, s19, 7\n"
        "s_sub_u32 s11, s18, s19\n"
        "s_and_b32 s10, s28, 3\n"
        "s_mov_b32 s14, 0\n"
        "s_mov_b32 s16, 0\n"
        "s_mov_b32 s17, -1\n"
        "s_cmp_ge_u32 s28, 4\n"
        "s_cbranch_scc1 .LG\n"
        "v_lshrrev_b32 v1, 2, v0\n"
        "v_and_b32 v1, 4, v1\n"
        "v_xor_b32 v1, v1, v0\n"
        "v_and_b32 v1, 7, v1\n"
        "v_lshlrev_b32 v1, 4, v1\n"
        "v_lshrrev_b32 v14, 3, v0\n"
        "v_mul_u32_u24 v14, 2688, v14\n"
        "v_add_u32 v14, v14, v1\n"
        "s_mul_i32 s21, s10, 43008\n"
        "s_lshl_b32 s19, s8, 7\n"
        "s_add_u32 s18, s21, s19\n"
        "s_add_u32 s18, s18, 896\n"
        "v_add_u32 v14, s18, v14\n"
        "v_add_u32 v15, 21504, v14\n"
        "s_lshl_b32 s70, s10, 11\n"
        "s_add_u32 s70, s70, 49152\n"
        "v_and_b32 v1, 3, v0\n"
        "v_lshlrev_b32 v2, 4, v1\n"
        "s_and_b32 s18, s8, 1\n"
        "s_lshl_b32 s19, s18, 3\n"
        "v_add_u32 v2, s19, v2\n"
        "v_cmp_eq_u32 vcc, 56, v2\n"
        "v_lshrrev_b32 v3, 2, v0\n"
        "v_mul_u32_u24 v3, 2688, v3\n"
        "v_cndmask_b32 v2, v2, 0, vcc\n"
        "s_mul_i32 s20, s8, 56\n"
        "s_add_u32 s19, s21, s20\n"
        "v_add3_u32 v236, v2, v3, s19\n"
        "s_lshl_b32 s80, s10, 10\n"
        "s_add_u32 s80, s80, 110592\n"
        "s_cmp_ge_u32 s8, 14\n"
        "s_waitcnt lgkmcnt(0)\n"
        "s_cbranch_scc1 .LA_nodma\n"
        "s_add_u32 m0, s70, 0\n"
        "s_mul_i32 s71, s11, 172032\n"
        "buffer_load_dwordx4 v14, s[36:39], s71 offen  lds\n"
        "s_add_u32 m0, s70, 1024\n"
        "s_nop 0\n"
        "buffer_load_dwordx4 v15, s[36:39], s71 offen  lds\n"
        "s_add_u32 m0, s80, 0\n"
        "s_nop 0\n"
        "buffer_load_dwordx4 v236, s[36:39], s71 offen  lds\n"
        "s_add_u32 s90, s11, 1\n"
        "s_cmp_eq_u32 s90, 7\n"
        "s_cselect_b32 s90, 0, s90\n"
        "s_add_u32 s91, s90, 1\n"
        "s_cmp_eq_u32 s91, 7\n"
        "s_cselect_b32 s91, 0, s91\n"
        "s_add_u32 s92, s91, 1\n"
        "s_cmp_eq_u32 s92, 7\n"
        "s_cselect_b32 s92, 0, s92\n"
        "s_add_u32 s93, s92, 1\n"
        "s_cmp_eq_u32 s93, 7\n"
        "s_cselect_b32 s93, 0, s93\n"
        "s_add_u32 s94, s93, 1\n"
        "s_cmp_eq_u32 s94, 7\n"
        "s_cselect_b32 s94, 0, s94\n"
        "s_add_u32 s95, s94, 1\n"
        "s_cmp_eq_u32 s95, 7\n"
        "s_cselect_b32 s95, 0, s95\n"
        "s_add_u32 m0, s70, 8192\n"
        "s_mul_i32 s71, s90, 172032\n"
        "buffer_load_dwordx4 v14, s[36:39], s71 offen  lds\n"
        "s_add_u32 m0, s70, 9216\n"
        "s_nop 0\n"
        "buffer_load_dwordx4 v15, s[36:39], s71 offen  lds\n"
        "s_add_u32 m0, s80, 4096\n"
        "s_nop 0\n"
        "buffer_load_dwordx4 v236, s[36:39], s71 offen  lds\n"
        "s_add_u32 m0, s70, 16384\n"
        "s_mul_i32 s71, s91, 172032\n"
        "buffer_load_dwordx4 v14, s[36:39], s71 offen  lds\n"
        "s_add_u32 m0, s70, 17408\n"
        "s_nop 0\n"
        "buffer_load_dwordx4 v15, s[36:39], s71 offen  lds\n"
        "s_add_u32 m0, s80, 8192\n"
        "s_nop 0\n"
        "buffer_load_dwordx4 v236, s[36:39], s71 offen  lds\n"
        "s_branch .LA_dma\n"
        ".LA_nodma:\n"
        "s_add_u32 m0, s80, 0\n"
        "s_mul_i32 s71, s11, 172032\n"
        "buffer_load_dwordx4 v236, s[36:39], s71 offen  lds\n"
        "s_add_u32 s90, s11, 1\n"
        "s_cmp_eq_u32 s90, 7\n"
        "s_cselect_b32 s90, 0, s90\n"
        "s_add_u32 s91, s90, 1\n"
        "s_cmp_eq_u32 s91, 7\n"
        "s_cselect_b32 s91, 0, s91\n"
        "s_add_u32 s92, s91, 1\n"
        "s_cmp_eq_u32 s92, 7\n"
        "s_cselect_b32 s92, 0, s92\n"
        "s_add_u32 s93, s92, 1\n"
        "s_cmp_eq_u32 s93, 7\n"
        "s_cselect_b32 s93, 0, s93\n"
        "s_add_u32 s94, s93, 1\n"
        "s_cmp_eq_u32 s94, 7\n"
        "s_cselect_b32 s94, 0, s94\n"
        "s_add_u32 s95, s94, 1\n"
        "s_cmp_eq_u32 s95, 7\n"
        "s_cselect_b32 s95, 0, s95\n"
        "s_add_u32 m0, s80, 4096\n"
        "s_mul_i32 s71, s90, 172032\n"
        "buffer_load_dwordx4 v236, s[36:39], s71 offen  lds\n"
        "s_add_u32 m0, s80, 8192\n"
        "s_mul_i32 s71, s91, 172032\n"
        "buffer_load_dwordx4 v236, s[36:39], s71 offen  lds\n"
        ".LA_dma:\n"
        "s_load_dwordx2 s[68:69], s[64:65], 0x0\n"
        "v_and_b32 v1, 31, v0\n"
        "v_bfe_u32 v2, v0, 1, 3\n"
        "v_lshrrev_b32 v8, 5, v0\n"
        "v_xor_b32 v2, v2, v8\n"
        "v_lshlrev_b32 v2, 4, v2\n"
        "v_lshl_or_b32 v8, v1, 7, v2\n"
        "s_lshl_b32 s18, s10, 12\n"
        "v_add_u32 v8, s18, v8\n"
        "v_xor_b32 v9, 32, v8\n"
        "v_xor_b32 v10, 64, v8\n"
        "v_xor_b32 v11, 0x60, v8\n"
        "v_and_b32 v6, 16, v0\n"
        "v_lshlrev_b32 v6, 1, v6\n"
        "v_and_b32 v1, 1, v0\n"
        "v_lshl_or_b32 v6, v1, 4, v6\n"
        "v_and_b32 v1, 2, v0\n"
        "v_lshl_or_b32 v6, v1, 2, v6\n"
        "v_bfe_u32 v7, v0, 2, 2\n"
        "v_lshrrev_b32 v1, 5, v0\n"
        "v_lshl_or_b32 v7, v1, 3, v7\n"
        "v_lshl_or_b32 v12, v7, 7, v6\n"
        "v_and_b32 v1, 8, v0\n"
        "v_lshl_or_b32 v12, v1, 3, v12\n"
        "v_add_u32 v12, 49152, v12\n"
        "v_xor_b32 v13, 64, v12\n"
        "v_lshl_or_b32 v237, v7, 6, v6\n"
        "v_add_u32 v237, 110592, v237\n"
        "v_lshlrev_b32 v240, 4, v0\n"
        "v_mov_b32 v241, 159744\n"
        "v_mov_b32 v242, 1\n"
        "v_mov_b32 v122, 0\n"
        "v_mov_b32 v123, 0\n"
        "v_mov_b32 v126, 0\n"
        "v_mov_b32 v127, 0\n"
        "v_mov_b32 v230, 0\n"
        "v_mov_b32 v231, 0\n"
        "s_lshl_b32 s72, s9, 2\n"
        "s_add_u32 s72, s72, s10\n"
        "s_setprio 2\n"
        "v_mov_b32 v16, 0\n"
        "v_mov_b32 v17, 0\n"
        "v_mov_b32 v18, 0\n"
        "v_mov_b32 v19, 0\n"
        "v_mov_b32 v20, 0\n"
        "v_mov_b32 v21, 0\n"
        "v_mov_b32 v22, 0\n"
        "v_mov_b32 v23, 0\n"
        "v_mov_b32 v24, 0\n"
        "v_mov_b32 v25, 0\n"
        "v_mov_b32 v26, 0\n"
        "v_mov_b32 v27, 0\n"
        "v_mov_b32 v28, 0\n"
        "v_mov_b32 v29, 0\n"
        "v_mov_b32 v30, 0\n"
        "v_mov_b32 v31, 0\n"
        "v_mov_b32 v32, 0\n"
        "v_mov_b32 v33, 0\n"
        "v_mov_b32 v34, 0\n"
        "v_mov_b32 v35, 0\n"
        "v_mov_b32 v36, 0\n"
        "v_mov_b32 v37, 0\n"
        "v_mov_b32 v38, 0\n"
        "v_mov_b32 v39, 0\n"
        "v_mov_b32 v40, 0\n"
        "v_mov_b32 v41, 0\n"
        "v_mov_b32 v42, 0\n"
        "v_mov_b32 v43, 0\n"
        "v_mov_b32 v44, 0\n"
        "v_mov_b32 v45, 0\n"
        "v_mov_b32 v46, 0\n"
        "v_mov_b32 v47, 0\n"
        "v_mov_b32 v48, 0\n"
        "v_mov_b32 v49, 0\n"
        "v_mov_b32 v50, 0\n"
        "v_mov_b32 v51, 0\n"
        "v_mov_b32 v52, 0\n"
        "v_mov_b32 v53, 0\n"
        "v_mov_b32 v54, 0\n"
        "v_mov_b32 v55, 0\n"
        "v_mov_b32 v56, 0\n"
        "v_mov_b32 v57, 0\n"
        "v_mov_b32 v58, 0\n"
        "v_mov_b32 v59, 0\n"
        "v_mov_b32 v60, 0\n"
        "v_mov_b32 v61, 0\n"
        "v_mov_b32 v62, 0\n"
        "v_mov_b32 v63, 0\n"
        "v_mov_b32 v64, 0\n"
        "v_mov_b32 v65, 0\n"
        "v_mov_b32 v66, 0\n"
        "v_mov_b32 v67, 0\n"
        "v_mov_b32 v68, 0\n"
        "v_mov_b32 v69, 0\n"
        "v_mov_b32 v70, 0\n"
        "v_mov_b32 v71, 0\n"
        "v_mov_b32 v72, 0\n"
        "v_mov_b32 v73, 0\n"
        "v_mov_b32 v74, 0\n"
        "v_mov_b32 v75, 0\n"
        "v_mov_b32 v76, 0\n"
        "v_mov_b32 v77, 0\n"
        "v_mov_b32 v78, 0\n"
        "v_mov_b32 v79, 0\n"
        "v_mov_b32 v112, 0\n"
        "v_mov_b32 v113, 0\n"
        "v_mov_b32 v114, 0\n"
        "v_mov_b32 v115, 0\n"
        "v_mov_b32 v116, 0\n"
        "v_mov_b32 v117, 0\n"
        "v_mov_b32 v118, 0\n"
        "v_mov_b32 v119, 0\n"
        "v_mov_b32 v220, 0\n"
        "v_mov_b32 v221, 0\n"
        "v_mov_b32 v222, 0\n"
        "v_mov_b32 v223, 0\n"
        "s_mul_i32 s18, s8, 7\n"
        "s_lshr_b32 s18, s18, 1\n"
        "s_and_b32 s19, s8, 1\n"
        "s_add_u32 s20, s18, s19\n"
        "s_lshl_b32 s20, s20, 9\n"
        "v_add_u32 v243, s20, v240\n"
        "v_add_u32 v244, 1024, v243\n"
        "s_mul_i32 s21, s19, 2040\n"
        "v_subrev_u32 v1, s21, v244\n"
        "v_cndmask_b32 v244, v244, v1, s[16:17]\n"
        "v_mov_b32 v245, 1065369472\n"
        "v_mov_b32 v246, 0x3f80\n"
        "s_mov_b32 s76, 0\n"
        "s_mov_b32 s77, 1115684864\n"
        "s_mul_i32 s18, s8, 28\n"
        "s_add_u32 s19, s18, 27\n"
        "s_lshr_b32 s18, s18, 6\n"
        "s_lshr_b32 s19, s19, 6\n"
        "s_sub_u32 s19, s19, s18\n"
        "v_lshrrev_b32 v2, 3, v0\n"
        "v_bfe_u32 v1, v0, 2, 1\n"
        "v_mul_u32_u24 v1, s19, v1\n"
        "v_cmp_eq_u32 vcc, 1, v2\n"
        "s_nop 1\n"
        "v_cndmask_b32 v2, 0, 7, vcc\n"
        "v_add3_u32 v1, v1, v2, s18\n"
        "v_and_b32 v2, 3, v0\n"
        "s_lshl_b32 s20, s10, 2\n"
        "v_add_u32 v2, s20, v2\n"
        "v_cmp_gt_u32 vcc, 16, v0\n"
        "v_lshlrev_b32 v1, 4, v1\n"
        "s_nop 0\n"
        "v_mov_b32 v3, s9\n"
        "v_cndmask_b32 v2, v3, v2, vcc\n"
        "v_add_lshl_u32 v247, v1, v2, 7\n"
        "v_add_u32 v247, 4096, v247\n"
        "s_add_u32 s18, s10, 1\n"
        "s_and_b32 s18, s18, 3\n"
        "s_lshl_b32 s19, s18, 11\n"
        "s_add_u32 s19, s19, 139264\n"
        "v_add_u32 v248, s19, v240\n"
        "s_mul_i32 s18, s18, 12288\n"
        "s_add_u32 s18, s18, 110592\n"
        "v_add_u32 v253, s18, v240\n"
        "s_add_u32 s18, s10, 2\n"
        "s_and_b32 s18, s18, 3\n"
        "s_lshl_b32 s19, s18, 11\n"
        "s_add_u32 s19, s19, 139264\n"
        "v_add_u32 v249, s19, v240\n"
        "s_mul_i32 s18, s18, 12288\n"
        "s_add_u32 s18, s18, 114688\n"
        "v_add_u32 v254, s18, v240\n"
        "s_add_u32 s18, s10, 3\n"
        "s_and_b32 s18, s18, 3\n"
        "s_lshl_b32 s19, s18, 11\n"
        "s_add_u32 s19, s19, 139264\n"
        "v_add_u32 v250, s19, v240\n"
        "s_mul_i32 s18, s18, 12288\n"
        "s_add_u32 s18, s18, 118784\n"
        "v_add_u32 v255, s18, v240\n"
        "s_add_u32 s18, s10, 4\n"
        "s_and_b32 s18, s18, 3\n"
        "s_lshl_b32 s19, s18, 11\n"
        "s_add_u32 s19, s19, 139264\n"
        "v_add_u32 v252, s19, v240\n"
        "s_mul_i32 s18, s18, 12288\n"
        "s_add_u32 s18, s18, 110592\n"
        "v_add_u32 v239, s18, v240\n"
        "v_mov_b32 v2, 0\n"
        "v_mov_b32 v3, 0\n"
        "s_cmp_lg_u32 s10, 0\n"
        "s_cbranch_scc1 .LA_nz\n"
        "s_mov_b64 exec, 1\n"
        "ds_write_b64 v241, v[2:3]\n"
        "ds_write_b64 v241, v[2:3] offset:8\n"
        "s_mov_b64 exec, -1\n"
        ".LA_nz:\n"
        "s_waitcnt lgkmcnt(0)\n"
        "s_mul_i32 s68, s68, 0x41C64E6D\n"
        "s_add_u32 s68, s68, 0x3039\n"
        "s_mul_i32 s69, s69, 0x0019660D\n"
        "s_add_u32 s69, s69, 0x3C6EF35F\n"
        "s_cmp_ge_u32 s8, 14\n"
        "s_cbranch_scc1 .LA_idle\n"
        "s_cmp_ge_u32 s8, 7\n"
        "s_cbranch_scc1 .LA_V\n"
        "s_waitcnt vmcnt(6)\n"
        "s_barrier\n"
        "ds_read_b128 v[80:83], v8 offset:0\n"
        "ds_read_b64_tr_b16 v[160:161], v12 offset:0\n"
        "ds_read_b64_tr_b16 v[162:163], v12 offset:512\n"
        "ds_read_b64_tr_b16 v[164:165], v13 offset:0\n"
        "ds_read_b64_tr_b16 v[166:167], v13 offset:512\n"
        "ds_read_b64_tr_b16 v[96:97], v237 offset:0\n"
        "ds_read_b64_tr_b16 v[98:99], v237 offset:256\n"
        "ds_read_b128 v[84:87], v9 offset:0\n"
        "ds_read_b64_tr_b16 v[168:169], v12 offset:2048\n"
        "ds_read_b64_tr_b16 v[170:171], v12 offset:2560\n"
        "ds_read_b64_tr_b16 v[172:173], v13 offset:2048\n"
        "ds_read_b64_tr_b16 v[174:175], v13 offset:2560\n"
        "ds_read_b64_tr_b16 v[100:101], v237 offset:1024\n"
        "ds_read_b64_tr_b16 v[102:103], v237 offset:1280\n"
        "s_waitcnt lgkmcnt(7)\n"
        "v_mfma_f32_32x32x16_bf16 v[128:143], v[160:163], v[80:83], 0\n"
        "ds_read_b128 v[88:91], v10 offset:0\n"
        "ds_read_b64_tr_b16 v[176:177], v12 offset:4096\n"
        "ds_read_b64_tr_b16 v[178:179], v12 offset:4608\n"
        "v_mfma_f32_32x32x16_bf16 v[144:159], v[164:167], v[80:83], 0\n"
        "ds_read_b64_tr_b16 v[180:181], v13 offset:4096\n"
        "ds_read_b64_tr_b16 v[182:183], v13 offset:4608\n"
        "v_mfma_f32_32x32x16_bf16 v[192:207], v[96:99], v[80:83], 0\n"
        "ds_read_b64_tr_b16 v[104:105], v237 offset:2048\n"
        "ds_read_b64_tr_b16 v[106:107], v237 offset:2304\n"
        "s_waitcnt lgkmcnt(7)\n"
        "v_mfma_f32_32x32x16_bf16 v[128:143], v[168:171], v[84:87], v[128:143]\n"
        "ds_read_b128 v[92:95], v11 offset:0\n"
        "ds_read_b64_tr_b16 v[184:185], v12 offset:6144\n"
        "ds_read_b64_tr_b16 v[186:187], v12 offset:6656\n"
        "v_mfma_f32_32x32x16_bf16 v[144:159], v[172:175], v[84:87], v[144:159]\n"
        "ds_read_b64_tr_b16 v[188:189], v13 offset:6144\n"
        "ds_read_b64_tr_b16 v[190:191], v13 offset:6656\n"
        "v_mfma_f32_32x32x16_bf16 v[192:207], v[100:103], v[84:87], v[192:207]\n"
        "ds_read_b64_tr_b16 v[108:109], v237 offset:3072\n"
        "ds_read_b64_tr_b16 v[110:111], v237 offset:3328\n"
        "s_waitcnt lgkmcnt(7)\n"
        "v_mfma_f32_32x32x16_bf16 v[128:143], v[176:179], v[88:91], v[128:143]\n"
        "v_mfma_f32_32x32x16_bf16 v[144:159], v[180:183], v[88:91], v[144:159]\n"
        "v_mfma_f32_32x32x16_bf16 v[192:207], v[104:107], v[88:91], v[192:207]\n"
        "s_waitcnt vmcnt(3)\n"
        "s_waitcnt lgkmcnt(0)\n"
        "s_barrier\n"
        "ds_read_b128 v[80:83], v8 offset:16384\n"
        "ds_read_b64_tr_b16 v[160:161], v12 offset:8192\n"
        "ds_read_b64_tr_b16 v[162:163], v12 offset:8704\n"
        "ds_read_b64_tr_b16 v[164:165], v13 offset:8192\n"
        "ds_read_b64_tr_b16 v[166:167], v13 offset:8704\n"
        "ds_read_b64_tr_b16 v[96:97], v237 offset:4096\n"
        "ds_read_b64_tr_b16 v[98:99], v237 offset:4352\n"
        "v_mfma_f32_32x32x16_bf16 v[128:143], v[184:187], v[92:95], v[128:143]\n"
        "ds_read_b128 v[84:87], v9 offset:16384\n"
        "ds_read_b64_tr_b16 v[168:169], v12 offset:10240\n"
        "ds_read_b64_tr_b16 v[170:171], v12 offset:10752\n"
        "ds_read_b64_tr_b16 v[172:173], v13 offset:10240\n"
        "v_mfma_f32_32x32x16_bf16 v[144:159], v[188:191], v[92:95], v[144:159]\n"
        "ds_read_b64_tr_b16 v[174:175], v13 offset:10752\n"
        "ds_read_b64_tr_b16 v[100:101], v237 offset:5120\n"
        "ds_read_b64_tr_b16 v[102:103], v237 offset:5376\n"
        "v_mfma_f32_32x32x16_bf16 v[192:207], v[108:111], v[92:95], v[192:207]\n"
        "s_add_u32 m0, s70, 0\n"
        "s_mul_i32 s71, s92, 172032\n"
        "buffer_load_dwordx4 v14, s[36:39], s71 offen  lds\n"
        "s_waitcnt lgkmcnt(7)\n"
        "v_mfma_f32_32x32x16_bf16 v[128:143], v[160:163], v[80:83], v[128:143]\n"
        "ds_read_b128 v[88:91], v10 offset:16384\n"
        "ds_read_b64_tr_b16 v[176:177], v12 offset:12288\n"
        "ds_read_b64_tr_b16 v[178:179], v12 offset:12800\n"
        "ds_read_b64_tr_b16 v[180:181], v13 offset:12288\n"
        "v_mfma_f32_32x32x16_bf16 v[144:159], v[164:167], v[80:83], v[144:159]\n"
        "ds_read_b64_tr_b16 v[182:183], v13 offset:12800\n"
        "ds_read_b64_tr_b16 v[104:105], v237 offset:6144\n"
        "ds_read_b64_tr_b16 v[106:107], v237 offset:6400\n"
        "v_mfma_f32_32x32x16_bf16 v[192:207], v[96:99], v[80:83], v[192:207]\n"
        "s_add_u32 m0, s70, 1024\n"
        "s_nop 0\n"
        "buffer_load_dwordx4 v15, s[36:39], s71 offen  lds\n"
        "s_waitcnt lgkmcnt(7)\n"
        "v_mfma_f32_32x32x16_bf16 v[128:143], v[168:171], v[84:87], v[128:143]\n"
        "ds_read_b128 v[92:95], v11 offset:16384\n"
        "ds_read_b64_tr_b16 v[184:185], v12 offset:14336\n"
        "ds_read_b64_tr_b16 v[186:187], v12 offset:14848\n"
        "ds_read_b64_tr_b16 v[188:189], v13 offset:14336\n"
        "v_mfma_f32_32x32x16_bf16 v[144:159], v[172:175], v[84:87], v[144:159]\n"
        "ds_read_b64_tr_b16 v[190:191], v13 offset:14848\n"
        "ds_read_b64_tr_b16 v[108:109], v237 offset:7168\n"
        "ds_read_b64_tr_b16 v[110:111], v237 offset:7424\n"
        "v_mfma_f32_32x32x16_bf16 v[192:207], v[100:103], v[84:87], v[192:207]\n"
        "s_add_u32 m0, s80, 12288\n"
        "s_nop 0\n"
        "buffer_load_dwordx4 v236, s[36:39], s71 offen  lds\n"
        "s_waitcnt lgkmcnt(7)\n"
        "v_mfma_f32_32x32x16_bf16 v[128:143], v[176:179], v[88:91], v[128:143]\n"
        "v_mfma_f32_32x32x16_bf16 v[144:159], v[180:183], v[88:91], v[144:159]\n"
        "v_mfma_f32_32x32x16_bf16 v[192:207], v[104:107], v[88:91], v[192:207]\n"
        "s_waitcnt vmcnt(3)\n"
        "s_waitcnt lgkmcnt(0)\n"
        "s_barrier\n"
        "ds_read_b128 v[80:83], v8 offset:32768\n"
        "ds_read_b64_tr_b16 v[160:161], v12 offset:16384\n"
        "ds_read_b64_tr_b16 v[162:163], v12 offset:16896\n"
        "ds_read_b64_tr_b16 v[164:165], v13 offset:16384\n"
        "ds_read_b64_tr_b16 v[166:167], v13 offset:16896\n"
        "ds_read_b64_tr_b16 v[96:97], v237 offset:8192\n"
        "ds_read_b64_tr_b16 v[98:99], v237 offset:8448\n"
        "v_mfma_f32_32x32x16_bf16 v[128:143], v[184:187], v[92:95], v[128:143]\n"
        "ds_read_b128 v[84:87], v9 offset:32768\n"
        "ds_read_b64_tr_b16 v[168:169], v12 offset:18432\n"
        "ds_read_b64_tr_b16 v[170:171], v12 offset:18944\n"
        "ds_read_b64_tr_b16 v[172:173], v13 offset:18432\n"
        "v_mfma_f32_32x32x16_bf16 v[144:159], v[188:191], v[92:95], v[144:159]\n"
        "ds_read_b64_tr_b16 v[174:175], v13 offset:18944\n"
        "ds_read_b64_tr_b16 v[100:101], v237 offset:9216\n"
        "ds_read_b64_tr_b16 v[102:103], v237 offset:9472\n"
        "v_mfma_f32_32x32x16_bf16 v[192:207], v[108:111], v[92:95], v[192:207]\n"
        "s_add_u32 m0, s70, 8192\n"
        "s_mul_i32 s71, s93, 172032\n"
        "buffer_load_dwordx4 v14, s[36:39], s71 offen  lds\n"
        "s_waitcnt lgkmcnt(7)\n"
        "v_mfma_f32_32x32x16_bf16 v[128:143], v[160:163], v[80:83], v[128:143]\n"
        "ds_read_b128 v[88:91], v10 offset:32768\n"
        "ds_read_b64_tr_b16 v[176:177], v12 offset:20480\n"
        "ds_read_b64_tr_b16 v[178:179], v12 offset:20992\n"
        "ds_read_b64_tr_b16 v[180:181], v13 offset:20480\n"
        "v_mfma_f32_32x32x16_bf16 v[144:159], v[164:167], v[80:83], v[144:159]\n"
        "ds_read_b64_tr_b16 v[182:183], v13 offset:20992\n"
        "ds_read_b64_tr_b16 v[104:105], v237 offset:10240\n"
        "ds_read_b64_tr_b16 v[106:107], v237 offset:10496\n"
        "v_mfma_f32_32x32x16_bf16 v[192:207], v[96:99], v[80:83], v[192:207]\n"
        "s_add_u32 m0, s70, 9216\n"
        "s_nop 0\n"
        "buffer_load_dwordx4 v15, s[36:39], s71 offen  lds\n"
        "s_waitcnt lgkmcnt(7)\n"
        "v_mfma_f32_32x32x16_bf16 v[128:143], v[168:171], v[84:87], v[128:143]\n"
        "ds_read_b128 v[92:95], v11 offset:32768\n"
        "ds_read_b64_tr_b16 v[184:185], v12 offset:22528\n"
        "ds_read_b64_tr_b16 v[186:187], v12 offset:23040\n"
        "ds_read_b64_tr_b16 v[188:189], v13 offset:22528\n"
        "v_mfma_f32_32x32x16_bf16 v[144:159], v[172:175], v[84:87], v[144:159]\n"
        "ds_read_b64_tr_b16 v[190:191], v13 offset:23040\n"
        "ds_read_b64_tr_b16 v[108:109], v237 offset:11264\n"
        "ds_read_b64_tr_b16 v[110:111], v237 offset:11520\n"
        "v_mfma_f32_32x32x16_bf16 v[192:207], v[100:103], v[84:87], v[192:207]\n"
        "s_add_u32 m0, s80, 16384\n"
        "s_nop 0\n"
        "buffer_load_dwordx4 v236, s[36:39], s71 offen  lds\n"
        "s_waitcnt lgkmcnt(7)\n"
        "v_mfma_f32_32x32x16_bf16 v[128:143], v[176:179], v[88:91], v[128:143]\n"
        "v_mfma_f32_32x32x16_bf16 v[144:159], v[180:183], v[88:91], v[144:159]\n"
        "v_mfma_f32_32x32x16_bf16 v[192:207], v[104:107], v[88:91], v[192:207]\n"
        "s_waitcnt vmcnt(3)\n"
        "s_waitcnt lgkmcnt(0)\n"
        "s_barrier\n"
        "ds_read_b128 v[80:83], v8 offset:0\n"
        "ds_read_b64_tr_b16 v[160:161], v12 offset:0\n"
        "ds_read_b64_tr_b16 v[162:163], v12 offset:512\n"
        "ds_read_b64_tr_b16 v[164:165], v13 offset:0\n"
        "ds_read_b64_tr_b16 v[166:167], v13 offset:512\n"
        "ds_read_b64_tr_b16 v[96:97], v237 offset:12288\n"
        "ds_read_b64_tr_b16 v[98:99], v237 offset:12544\n"
        "v_mfma_f32_32x32x16_bf16 v[128:143], v[184:187], v[92:95], v[128:143]\n"
        "ds_read_b128 v[84:87], v9 offset:0\n"
        "ds_read_b64_tr_b16 v[168:169], v12 offset:2048\n"
        "ds_read_b64_tr_b16 v[170:171], v12 offset:2560\n"
        "ds_read_b64_tr_b16 v[172:173], v13 offset:2048\n"
        "v_mfma_f32_32x32x16_bf16 v[144:159], v[188:191], v[92:95], v[144:159]\n"
        "ds_read_b64_tr_b16 v[174:175], v13 offset:2560\n"
        "ds_read_b64_tr_b16 v[100:101], v237 offset:13312\n"
        "ds_read_b64_tr_b16 v[102:103], v237 offset:13568\n"
        "v_mfma_f32_32x32x16_bf16 v[192:207], v[108:111], v[92:95], v[192:207]\n"
        "s_add_u32 m0, s70, 16384\n"
        "s_mul_i32 s71, s94, 172032\n"
        "buffer_load_dwordx4 v14, s[36:39], s71 offen  lds\n"
        "s_waitcnt lgkmcnt(7)\n"
        "v_mfma_f32_32x32x16_bf16 v[128:143], v[160:163], v[80:83], v[128:143]\n"
        "ds_read_b128 v[88:91], v10 offset:0\n"
        "ds_read_b64_tr_b16 v[176:177], v12 offset:4096\n"
        "ds_read_b64_tr_b16 v[178:179], v12 offset:4608\n"
        "ds_read_b64_tr_b16 v[180:181], v13 offset:4096\n"
        "v_mfma_f32_32x32x16_bf16 v[144:159], v[164:167], v[80:83], v[144:159]\n"
        "ds_read_b64_tr_b16 v[182:183], v13 offset:4608\n"
        "ds_read_b64_tr_b16 v[104:105], v237 offset:14336\n"
        "ds_read_b64_tr_b16 v[106:107], v237 offset:14592\n"
        "v_mfma_f32_32x32x16_bf16 v[192:207], v[96:99], v[80:83], v[192:207]\n"
        "s_add_u32 m0, s70, 17408\n"
        "s_nop 0\n"
        "buffer_load_dwordx4 v15, s[36:39], s71 offen  lds\n"
        "s_waitcnt lgkmcnt(7)\n"
        "v_mfma_f32_32x32x16_bf16 v[128:143], v[168:171], v[84:87], v[128:143]\n"
        "ds_read_b128 v[92:95], v11 offset:0\n"
        "ds_read_b64_tr_b16 v[184:185], v12 offset:6144\n"
        "ds_read_b64_tr_b16 v[186:187], v12 offset:6656\n"
        "ds_read_b64_tr_b16 v[188:189], v13 offset:6144\n"
        "v_mfma_f32_32x32x16_bf16 v[144:159], v[172:175], v[84:87], v[144:159]\n"
        "ds_read_b64_tr_b16 v[190:191], v13 offset:6656\n"
        "ds_read_b64_tr_b16 v[108:109], v237 offset:15360\n"
        "ds_read_b64_tr_b16 v[110:111], v237 offset:15616\n"
        "v_mfma_f32_32x32x16_bf16 v[192:207], v[100:103], v[84:87], v[192:207]\n"
        "s_add_u32 m0, s80, 20480\n"
        "s_nop 0\n"
        "buffer_load_dwordx4 v236, s[36:39], s71 offen  lds\n"
        "s_waitcnt lgkmcnt(7)\n"
        "v_mfma_f32_32x32x16_bf16 v[128:143], v[176:179], v[88:91], v[128:143]\n"
        "v_mfma_f32_32x32x16_bf16 v[144:159], v[180:183], v[88:91], v[144:159]\n"
        "v_mfma_f32_32x32x16_bf16 v[192:207], v[104:107], v[88:91], v[192:207]\n"
        "s_waitcnt vmcnt(3)\n"
        "s_waitcnt lgkmcnt(0)\n"
        "s_barrier\n"
        "ds_read_b128 v[80:83], v8 offset:16384\n"
        "ds_read_b64_tr_b16 v[160:161], v12 offset:8192\n"
        "ds_read_b64_tr_b16 v[162:163], v12 offset:8704\n"
        "ds_read_b64_tr_b16 v[164:165], v13 offset:8192\n"
        "ds_read_b64_tr_b16 v[166:167], v13 offset:8704\n"
        "ds_read_b64_tr_b16 v[96:97], v237 offset:16384\n"
        "ds_read_b64_tr_b16 v[98:99], v237 offset:16640\n"
        "v_mfma_f32_32x32x16_bf16 v[128:143], v[184:187], v[92:95], v[128:143]\n"
        "ds_read_b128 v[84:87], v9 offset:16384\n"
        "ds_read_b64_tr_b16 v[168:169], v12 offset:10240\n"
        "ds_read_b64_tr_b16 v[170:171], v12 offset:10752\n"
        "ds_read_b64_tr_b16 v[172:173], v13 offset:10240\n"
        "v_mfma_f32_32x32x16_bf16 v[144:159], v[188:191], v[92:95], v[144:159]\n"
        "ds_read_b64_tr_b16 v[174:175], v13 offset:10752\n"
        "ds_read_b64_tr_b16 v[100:101], v237 offset:17408\n"
        "ds_read_b64_tr_b16 v[102:103], v237 offset:17664\n"
        "v_mfma_f32_32x32x16_bf16 v[192:207], v[108:111], v[92:95], v[192:207]\n"
        "s_add_u32 m0, s70, 0\n"
        "s_mul_i32 s71, s95, 172032\n"
        "buffer_load_dwordx4 v14, s[36:39], s71 offen  lds\n"
        "s_waitcnt lgkmcnt(7)\n"
        "v_mfma_f32_32x32x16_bf16 v[128:143], v[160:163], v[80:83], v[128:143]\n"
        "ds_read_b128 v[88:91], v10 offset:16384\n"
        "ds_read_b64_tr_b16 v[176:177], v12 offset:12288\n"
        "ds_read_b64_tr_b16 v[178:179], v12 offset:12800\n"
        "ds_read_b64_tr_b16 v[180:181], v13 offset:12288\n"
        "v_mfma_f32_32x32x16_bf16 v[144:159], v[164:167], v[80:83], v[144:159]\n"
        "ds_read_b64_tr_b16 v[182:183], v13 offset:12800\n"
        "ds_read_b64_tr_b16 v[104:105], v237 offset:18432\n"
        "ds_read_b64_tr_b16 v[106:107], v237 offset:18688\n"
        "v_mfma_f32_32x32x16_bf16 v[192:207], v[96:99], v[80:83], v[192:207]\n"
        "s_add_u32 m0, s70, 1024\n"
        "s_nop 0\n"
        "buffer_load_dwordx4 v15, s[36:39], s71 offen  lds\n"
        "s_waitcnt lgkmcnt(7)\n"
        "v_mfma_f32_32x32x16_bf16 v[128:143], v[168:171], v[84:87], v[128:143]\n"
        "ds_read_b128 v[92:95], v11 offset:16384\n"
        "ds_read_b64_tr_b16 v[184:185], v12 offset:14336\n"
        "ds_read_b64_tr_b16 v[186:187], v12 offset:14848\n"
        "ds_read_b64_tr_b16 v[188:189], v13 offset:14336\n"
        "v_mfma_f32_32x32x16_bf16 v[144:159], v[172:175], v[84:87], v[144:159]\n"
        "ds_read_b64_tr_b16 v[190:191], v13 offset:14848\n"
        "ds_read_b64_tr_b16 v[108:109], v237 offset:19456\n"
        "ds_read_b64_tr_b16 v[110:111], v237 offset:19712\n"
        "v_mfma_f32_32x32x16_bf16 v[192:207], v[100:103], v[84:87], v[192:207]\n"
        "s_add_u32 m0, s80, 24576\n"
        "s_nop 0\n"
        "buffer_load_dwordx4 v236, s[36:39], s71 offen  lds\n"
        "s_waitcnt lgkmcnt(7)\n"
        "v_mfma_f32_32x32x16_bf16 v[128:143], v[176:179], v[88:91], v[128:143]\n"
        "v_mfma_f32_32x32x16_bf16 v[144:159], v[180:183], v[88:91], v[144:159]\n"
        "v_mfma_f32_32x32x16_bf16 v[192:207], v[104:107], v[88:91], v[192:207]\n"
        "s_waitcnt vmcnt(3)\n"
        "s_waitcnt lgkmcnt(0)\n"
        "s_barrier\n"
        "ds_read_b128 v[80:83], v8 offset:32768\n"
        "ds_read_b64_tr_b16 v[160:161], v12 offset:16384\n"
        "ds_read_b64_tr_b16 v[162:163], v12 offset:16896\n"
        "ds_read_b64_tr_b16 v[164:165], v13 offset:16384\n"
        "ds_read_b64_tr_b16 v[166:167], v13 offset:16896\n"
        "ds_read_b64_tr_b16 v[96:97], v237 offset:20480\n"
        "ds_read_b64_tr_b16 v[98:99], v237 offset:20736\n"
        "v_mfma_f32_32x32x16_bf16 v[128:143], v[184:187], v[92:95], v[128:143]\n"
        "ds_read_b128 v[84:87], v9 offset:32768\n"
        "ds_read_b64_tr_b16 v[168:169], v12 offset:18432\n"
        "ds_read_b64_tr_b16 v[170:171], v12 offset:18944\n"
        "v_mfma_f32_32x32x16_bf16 v[144:159], v[188:191], v[92:95], v[144:159]\n"
        "ds_read_b64_tr_b16 v[172:173], v13 offset:18432\n"
        "ds_read_b64_tr_b16 v[174:175], v13 offset:18944\n"
        "v_mfma_f32_32x32x16_bf16 v[192:207], v[108:111], v[92:95], v[192:207]\n"
        "ds_read_b64_tr_b16 v[100:101], v237 offset:21504\n"
        "ds_read_b64_tr_b16 v[102:103], v237 offset:21760\n"
        "s_waitcnt lgkmcnt(7)\n"
        "v_mfma_f32_32x32x16_bf16 v[128:143], v[160:163], v[80:83], v[128:143]\n"
        "ds_read_b128 v[88:91], v10 offset:32768\n"
        "ds_read_b64_tr_b16 v[176:177], v12 offset:20480\n"
        "ds_read_b64_tr_b16 v[178:179], v12 offset:20992\n"
        "v_mfma_f32_32x32x16_bf16 v[144:159], v[164:167], v[80:83], v[144:159]\n"
        "ds_read_b64_tr_b16 v[180:181], v13 offset:20480\n"
        "ds_read_b64_tr_b16 v[182:183], v13 offset:20992\n"
        "v_mfma_f32_32x32x16_bf16 v[192:207], v[96:99], v[80:83], v[192:207]\n"
        "ds_read_b64_tr_b16 v[104:105], v237 offset:22528\n"
        "ds_read_b64_tr_b16 v[106:107], v237 offset:22784\n"
        "s_waitcnt lgkmcnt(7)\n"
        "v_mfma_f32_32x32x16_bf16 v[128:143], v[168:171], v[84:87], v[128:143]\n"
        "ds_read_b128 v[92:95], v11 offset:32768\n"
        "ds_read_b64_tr_b16 v[184:185], v12 offset:22528\n"
        "ds_read_b64_tr_b16 v[186:187], v12 offset:23040\n"
        "v_mfma_f32_32x32x16_bf16 v[144:159], v[172:175], v[84:87], v[144:159]\n"
        "ds_read_b64_tr_b16 v[188:189], v13 offset:22528\n"
        "ds_read_b64_tr_b16 v[190:191], v13 offset:23040\n"
        "v_mfma_f32_32x32x16_bf16 v[192:207], v[100:103], v[84:87], v[192:207]\n"
        "ds_read_b64_tr_b16 v[108:109], v237 offset:23552\n"
        "ds_read_b64_tr_b16 v[110:111], v237 offset:23808\n"
        "s_waitcnt lgkmcnt(7)\n"
        "v_mfma_f32_32x32x16_bf16 v[128:143], v[176:179], v[88:91], v[128:143]\n"
        "v_mfma_f32_32x32x16_bf16 v[144:159], v[180:183], v[88:91], v[144:159]\n"
        "v_mfma_f32_32x32x16_bf16 v[192:207], v[104:107], v[88:91], v[192:207]\n"
        "s_waitcnt vmcnt(0)\n"
        "s_waitcnt lgkmcnt(0)\n"
        "s_barrier\n"
        "ds_read_b128 v[80:83], v8 offset:0\n"
        "ds_read_b64_tr_b16 v[160:161], v12 offset:0\n"
        "ds_read_b64_tr_b16 v[162:163], v12 offset:512\n"
        "ds_read_b64_tr_b16 v[164:165], v13 offset:0\n"
        "ds_read_b64_tr_b16 v[166:167], v13 offset:512\n"
        "ds_read_b64_tr_b16 v[96:97], v237 offset:24576\n"
        "ds_read_b64_tr_b16 v[98:99], v237 offset:24832\n"
        "v_mfma_f32_32x32x16_bf16 v[128:143], v[184:187], v[92:95], v[128:143]\n"
        "ds_read_b128 v[84:87], v9 offset:0\n"
        "ds_read_b64_tr_b16 v[168:169], v12 offset:2048\n"
        "ds_read_b64_tr_b16 v[170:171], v12 offset:2560\n"
        "v_mfma_f32_32x32x16_bf16 v[144:159], v[188:191], v[92:95], v[144:159]\n"
        "ds_read_b64_tr_b16 v[172:173], v13 offset:2048\n"
        "ds_read_b64_tr_b16 v[174:175], v13 offset:2560\n"
        "v_mfma_f32_32x32x16_bf16 v[192:207], v[108:111], v[92:95], v[192:207]\n"
        "ds_read_b64_tr_b16 v[100:101], v237 offset:25600\n"
        "ds_read_b64_tr_b16 v[102:103], v237 offset:25856\n"
        "s_waitcnt lgkmcnt(7)\n"
        "v_mfma_f32_32x32x16_bf16 v[128:143], v[160:163], v[80:83], v[128:143]\n"
        "ds_read_b128 v[88:91], v10 offset:0\n"
        "ds_read_b64_tr_b16 v[176:177], v12 offset:4096\n"
        "ds_read_b64_tr_b16 v[178:179], v12 offset:4608\n"
        "v_mfma_f32_32x32x16_bf16 v[144:159], v[164:167], v[80:83], v[144:159]\n"
        "ds_read_b64_tr_b16 v[180:181], v13 offset:4096\n"
        "ds_read_b64_tr_b16 v[182:183], v13 offset:4608\n"
        "v_mfma_f32_32x32x16_bf16 v[192:207], v[96:99], v[80:83], v[192:207]\n"
        "ds_read_b64_tr_b16 v[104:105], v237 offset:26624\n"
        "ds_read_b64_tr_b16 v[106:107], v237 offset:26880\n"
        "s_waitcnt lgkmcnt(7)\n"
        "v_mfma_f32_32x32x16_bf16 v[128:143], v[168:171], v[84:87], v[128:143]\n"
        "ds_read_b128 v[92:95], v11 offset:0\n"
        "ds_read_b64_tr_b16 v[184:185], v12 offset:6144\n"
        "ds_read_b64_tr_b16 v[186:187], v12 offset:6656\n"
        "v_mfma_f32_32x32x16_bf16 v[144:159], v[172:175], v[84:87], v[144:159]\n"
        "ds_read_b64_tr_b16 v[188:189], v13 offset:6144\n"
        "ds_read_b64_tr_b16 v[190:191], v13 offset:6656\n"
        "v_mfma_f32_32x32x16_bf16 v[192:207], v[100:103], v[84:87], v[192:207]\n"
        "ds_read_b64_tr_b16 v[108:109], v237 offset:27648\n"
        "ds_read_b64_tr_b16 v[110:111], v237 offset:27904\n"
        "s_waitcnt lgkmcnt(7)\n"
        "v_mfma_f32_32x32x16_bf16 v[128:143], v[176:179], v[88:91], v[128:143]\n"
        "v_mfma_f32_32x32x16_bf16 v[144:159], v[180:183], v[88:91], v[144:159]\n"
        "v_mfma_f32_32x32x16_bf16 v[192:207], v[104:107], v[88:91], v[192:207]\n"
        "v_and_b32 v1, 31, v0\n"
        "v_lshlrev_b32 v1, 4, v1\n"
        "s_lshl_b32 s18, s10, 9\n"
        "s_add_u32 s18, s18, 160768\n"
        "v_add_u32 v1, s18, v1\n"
        "v_add_u32 v3, 106496, v240\n"
        "s_waitcnt lgkmcnt(0)\n"
        "s_barrier\n"
        "ds_read_b128 v[208:211], v3\n"
        "ds_read_b128 v[212:215], v3 offset:1024\n"
        "ds_read_b128 v[224:227], v1\n"
        "ds_read_b128 v[216:219], v3 offset:2048\n"
        "v_mfma_f32_32x32x16_bf16 v[128:143], v[184:187], v[92:95], v[128:143]\n"
        "v_mfma_f32_32x32x16_bf16 v[144:159], v[188:191], v[92:95], v[144:159]\n"
        "v_mfma_f32_32x32x16_bf16 v[192:207], v[108:111], v[92:95], v[192:207]\n"
        "s_waitcnt lgkmcnt(2)\n"
        "v_pk_add_f32 v[208:209], v[208:209], v[210:211]\n"
        "v_add_f32 v208, v208, v209\n"
        "v_pk_add_f32 v[212:213], v[212:213], v[214:215]\n"
        "v_add_f32 v212, v212, v213\n"
        "v_cvt_pk_bf16_f32 v3, v208, v208\n"
        "v_and_b32 v4, 0xffff0000, v3\n"
        "v_sub_f32 v4, v208, v4\n"
        "v_cvt_pk_bf16_f32 v120, v208, v4\n"
        "v_and_b32 v121, 0xffff, v3\n"
        "v_cvt_pk_bf16_f32 v5, v212, v212\n"
        "v_and_b32 v6, 0xffff0000, v5\n"
        "v_sub_f32 v6, v212, v6\n"
        "v_cvt_pk_bf16_f32 v124, v212, v6\n"
        "v_and_b32 v125, 0xffff, v5\n"
        "s_waitcnt lgkmcnt(0)\n"
        "v_mov_b32 v234, v227\n"
        "v_mov_b32 v227, 0\n"
        "s_nop 1\n"
        "v_mfma_f32_32x32x16_bf16 v[128:143], v[120:123], v[224:227], v[128:143]\n"
        "v_mfma_f32_32x32x16_bf16 v[144:159], v[124:127], v[224:227], v[144:159]\n"
        "v_pk_add_f32 v[216:217], v[216:217], v[218:219]\n"
        "v_add_f32 v216, v216, v217\n"
        "v_cvt_pk_bf16_f32 v3, v216, v216\n"
        "v_and_b32 v4, 0xffff0000, v3\n"
        "v_sub_f32 v4, v216, v4\n"
        "v_cvt_pk_bf16_f32 v228, v216, v4\n"
        "v_and_b32 v229, 0xffff, v3\n"
        "s_mul_i32 s18, s72, 28672\n"
        "s_lshl_b32 s19, s8, 12\n"
        "s_add_u32 s18, s18, s19\n"
        "s_add_u32 s18, s18, 131072\n"
        "v_mfma_f32_32x32x16_bf16 v[192:207], v[228:231], v[224:227], v[192:207]\n"
        "v_pk_mul_f32 v[128:129], v[128:129], v[234:235] op_sel_hi:[1,0]\n"
        "v_pk_mul_f32 v[130:131], v[130:131], v[234:235] op_sel_hi:[1,0]\n"
        "v_pk_mul_f32 v[132:133], v[132:133], v[234:235] op_sel_hi:[1,0]\n"
        "v_pk_mul_f32 v[134:135], v[134:135], v[234:235] op_sel_hi:[1,0]\n"
        "v_cvt_pk_bf16_f32 v176, v128, v129\n"
        "v_cvt_pk_bf16_f32 v177, v130, v131\n"
        "v_cvt_pk_bf16_f32 v178, v132, v133\n"
        "v_cvt_pk_bf16_f32 v179, v134, v135\n"
        "buffer_store_dwordx4 v[176:179], v240, s[64:67], s18 offen offset:0 sc1\n"
        "v_pk_mul_f32 v[136:137], v[136:137], v[234:235] op_sel_hi:[1,0]\n"
        "v_pk_mul_f32 v[138:139], v[138:139], v[234:235] op_sel_hi:[1,0]\n"
        "v_pk_mul_f32 v[140:141], v[140:141], v[234:235] op_sel_hi:[1,0]\n"
        "v_pk_mul_f32 v[142:143], v[142:143], v[234:235] op_sel_hi:[1,0]\n"
        "v_cvt_pk_bf16_f32 v180, v136, v137\n"
        "v_cvt_pk_bf16_f32 v181, v138, v139\n"
        "v_cvt_pk_bf16_f32 v182, v140, v141\n"
        "v_cvt_pk_bf16_f32 v183, v142, v143\n"
        "buffer_store_dwordx4 v[180:183], v240, s[64:67], s18 offen offset:1024 sc1\n"
        "v_pk_mul_f32 v[144:145], v[144:145], v[234:235] op_sel_hi:[1,0]\n"
        "v_pk_mul_f32 v[146:147], v[146:147], v[234:235] op_sel_hi:[1,0]\n"
        "v_pk_mul_f32 v[148:149], v[148:149], v[234:235] op_sel_hi:[1,0]\n"
        "v_pk_mul_f32 v[150:151], v[150:151], v[234:235] op_sel_hi:[1,0]\n"
        "v_cvt_pk_bf16_f32 v184, v144, v145\n"
        "v_cvt_pk_bf16_f32 v185, v146, v147\n"
        "v_cvt_pk_bf16_f32 v186, v148, v149\n"
        "v_cvt_pk_bf16_f32 v187, v150, v151\n"
        "buffer_store_dwordx4 v[184:187], v240, s[64:67], s18 offen offset:2048 sc1\n"
        "v_pk_mul_f32 v[152:153], v[152:153], v[234:235] op_sel_hi:[1,0]\n"
        "v_pk_mul_f32 v[154:155], v[154:155], v[234:235] op_sel_hi:[1,0]\n"
        "v_pk_mul_f32 v[156:157], v[156:157], v[234:235] op_sel_hi:[1,0]\n"
        "v_pk_mul_f32 v[158:159], v[158:159], v[234:235] op_sel_hi:[1,0]\n"
        "v_cvt_pk_bf16_f32 v188, v152, v153\n"
        "v_cvt_pk_bf16_f32 v189, v154, v155\n"
        "v_cvt_pk_bf16_f32 v190, v156, v157\n"
        "v_cvt_pk_bf16_f32 v191, v158, v159\n"
        "buffer_store_dwordx4 v[188:191], v240, s[64:67], s18 offen offset:3072 sc1\n"
        "s_branch .LA_q\n"
        ".LA_V:\n"
        "s_sub_u32 s20, s8, 7\n"
        "s_lshl_b32 s20, s20, 6\n"
        "v_and_b32 v1, 31, v0\n"
        "v_lshrrev_b32 v2, 1, v1\n"
        "v_xor_b32 v2, v2, v1\n"
        "v_and_b32 v2, 4, v2\n"
        "v_lshl_or_b32 v2, v2, 1, v2\n"
        "v_xor_b32 v1, v1, v2\n"
        "v_add_u32 v1, s20, v1\n"
        "v_add_u32 v2, 0, v1\n"
        "v_mul_u32_u24 v3, 2341, v2\n"
        "v_lshrrev_b32 v3, 16, v3\n"
        "v_mul_u32_u24 v4, 28, v3\n"
        "v_sub_u32 v4, v2, v4\n"
        "v_subrev_u32 v5, 4, v4\n"
        "v_add_u32 v6, 24, v4\n"
        "v_cmp_gt_u32 vcc, 4, v4\n"
        "v_and_b32 v7, 1, v3\n"
        "s_nop 0\n"
        "v_cndmask_b32 v5, v5, v6, vcc\n"
        "v_cmp_eq_u32 vcc, 1, v7\n"
        "s_nop 1\n"
        "v_cndmask_b32 v4, v4, v5, vcc\n"
        "v_lshrrev_b32 v5, 1, v4\n"
        "v_xor_b32 v5, v5, v4\n"
        "v_and_b32 v5, 4, v5\n"
        "v_lshl_or_b32 v5, v5, 1, v5\n"
        "v_xor_b32 v4, v4, v5\n"
        "v_lshlrev_b32 v4, 4, v4\n"
        "v_lshl_or_b32 v4, v3, 17, v4\n"
        "v_lshrrev_b32 v5, 5, v0\n"
        "v_lshl_or_b32 v232, v5, 9, v4\n"
        "v_add_u32 v2, 32, v1\n"
        "v_mul_u32_u24 v3, 2341, v2\n"
        "v_lshrrev_b32 v3, 16, v3\n"
        "v_mul_u32_u24 v4, 28, v3\n"
        "v_sub_u32 v4, v2, v4\n"
        "v_subrev_u32 v5, 4, v4\n"
        "v_add_u32 v6, 24, v4\n"
        "v_cmp_gt_u32 vcc, 4, v4\n"
        "v_and_b32 v7, 1, v3\n"
        "s_nop 0\n"
        "v_cndmask_b32 v5, v5, v6, vcc\n"
        "v_cmp_eq_u32 vcc, 1, v7\n"
        "s_nop 1\n"
        "v_cndmask_b32 v4, v4, v5, vcc\n"
        "v_lshrrev_b32 v5, 1, v4\n"
        "v_xor_b32 v5, v5, v4\n"
        "v_and_b32 v5, 4, v5\n"
        "v_lshl_or_b32 v5, v5, 1, v5\n"
        "v_xor_b32 v4, v4, v5\n"
        "v_lshlrev_b32 v4, 4, v4\n"
        "v_lshl_or_b32 v4, v3, 17, v4\n"
        "v_lshrrev_b32 v5, 5, v0\n"
        "v_lshl_or_b32 v233, v5, 9, v4\n"
        "s_mul_i32 s21, s20, 2341\n"
        "s_lshr_b32 s21, s21, 16\n"
        "s_add_u32 s20, s20, 37\n"
        "v_lshrrev_b32 v1, 4, v0\n"
        "v_add_u32 v1, s21, v1\n"
        "v_mul_u32_u24 v2, 28, v1\n"
        "v_cmp_gt_u32 vcc, s20, v2\n"
        "v_and_b32 v3, 3, v0\n"
        "v_add_u32 v4, 28, v3\n"
        "v_lshlrev_b32 v4, 4, v4\n"
        "v_bfe_u32 v5, v0, 2, 2\n"
        "v_lshl_or_b32 v4, v5, 9, v4\n"
        "v_lshl_or_b32 v4, v1, 17, v4\n"
        "v_mov_b32 v5, 1073741824\n"
        "v_cndmask_b32 v4, v5, v4, vcc\n"
        "v_cmp_eq_u32 vcc, 0, v3\n"
        "v_mov_b32 v5, 1065369472\n"
        "s_lshl_b32 s18, s72, 11\n"
        "v_cndmask_b32 v208, 0, v5, vcc\n"
        "v_cndmask_b32 v209, 0, v5, vcc\n"
        "v_cndmask_b32 v210, 0, v5, vcc\n"
        "v_cndmask_b32 v211, 0, v5, vcc\n"
        "s_add_u32 s18, s18, 2097152\n"
        "buffer_store_dwordx4 v[208:211], v4, s[64:67], s18 offen sc1\n"
        "s_waitcnt vmcnt(7)\n"
        "s_barrier\n"
        "ds_read_b128 v[80:83], v8 offset:0\n"
        "ds_read_b64_tr_b16 v[160:161], v12 offset:0\n"
        "ds_read_b64_tr_b16 v[162:163], v12 offset:512\n"
        "ds_read_b64_tr_b16 v[164:165], v13 offset:0\n"
        "ds_read_b64_tr_b16 v[166:167], v13 offset:512\n"
        "ds_read_b64_tr_b16 v[96:97], v237 offset:0\n"
        "ds_read_b64_tr_b16 v[98:99], v237 offset:256\n"
        "ds_read_b128 v[84:87], v9 offset:0\n"
        "ds_read_b64_tr_b16 v[168:169], v12 offset:2048\n"
        "ds_read_b64_tr_b16 v[170:171], v12 offset:2560\n"
        "ds_read_b64_tr_b16 v[172:173], v13 offset:2048\n"
        "ds_read_b64_tr_b16 v[174:175], v13 offset:2560\n"
        "ds_read_b64_tr_b16 v[100:101], v237 offset:1024\n"
        "ds_read_b64_tr_b16 v[102:103], v237 offset:1280\n"
        "s_waitcnt lgkmcnt(7)\n"
        "v_mfma_f32_32x32x16_bf16 v[128:143], v[80:83], v[160:163], 0\n"
        "ds_read_b128 v[88:91], v10 offset:0\n"
        "ds_read_b64_tr_b16 v[176:177], v12 offset:4096\n"
        "ds_read_b64_tr_b16 v[178:179], v12 offset:4608\n"
        "v_mfma_f32_32x32x16_bf16 v[144:159], v[80:83], v[164:167], 0\n"
        "ds_read_b64_tr_b16 v[180:181], v13 offset:4096\n"
        "ds_read_b64_tr_b16 v[182:183], v13 offset:4608\n"
        "v_mfma_f32_32x32x16_bf16 v[192:207], v[96:99], v[80:83], 0\n"
        "ds_read_b64_tr_b16 v[104:105], v237 offset:2048\n"
        "ds_read_b64_tr_b16 v[106:107], v237 offset:2304\n"
        "s_waitcnt lgkmcnt(7)\n"
        "v_mfma_f32_32x32x16_bf16 v[128:143], v[84:87], v[168:171], v[128:143]\n"
        "ds_read_b128 v[92:95], v11 offset:0\n"
        "ds_read_b64_tr_b16 v[184:185], v12 offset:6144\n"
        "ds_read_b64_tr_b16 v[186:187], v12 offset:6656\n"
        "v_mfma_f32_32x32x16_bf16 v[144:159], v[84:87], v[172:175], v[144:159]\n"
        "ds_read_b64_tr_b16 v[188:189], v13 offset:6144\n"
        "ds_read_b64_tr_b16 v[190:191], v13 offset:6656\n"
        "v_mfma_f32_32x32x16_bf16 v[192:207], v[100:103], v[84:87], v[192:207]\n"
        "ds_read_b64_tr_b16 v[108:109], v237 offset:3072\n"
        "ds_read_b64_tr_b16 v[110:111], v237 offset:3328\n"
        "s_waitcnt lgkmcnt(7)\n"
        "v_mfma_f32_32x32x16_bf16 v[128:143], v[88:91], v[176:179], v[128:143]\n"
        "v_mfma_f32_32x32x16_bf16 v[144:159], v[88:91], v[180:183], v[144:159]\n"
        "v_mfma_f32_32x32x16_bf16 v[192:207], v[104:107], v[88:91], v[192:207]\n"
        "s_waitcnt vmcnt(4)\n"
        "s_waitcnt lgkmcnt(0)\n"
        "s_barrier\n"
        "ds_read_b128 v[80:83], v8 offset:16384\n"
        "ds_read_b64_tr_b16 v[160:161], v12 offset:8192\n"
        "ds_read_b64_tr_b16 v[162:163], v12 offset:8704\n"
        "ds_read_b64_tr_b16 v[164:165], v13 offset:8192\n"
        "ds_read_b64_tr_b16 v[166:167], v13 offset:8704\n"
        "ds_read_b64_tr_b16 v[96:97], v237 offset:4096\n"
        "ds_read_b64_tr_b16 v[98:99], v237 offset:4352\n"
        "v_mfma_f32_32x32x16_bf16 v[128:143], v[92:95], v[184:187], v[128:143]\n"
        "ds_read_b128 v[84:87], v9 offset:16384\n"
        "ds_read_b64_tr_b16 v[168:169], v12 offset:10240\n"
        "ds_read_b64_tr_b16 v[170:171], v12 offset:10752\n"
        "ds_read_b64_tr_b16 v[172:173], v13 offset:10240\n"
        "v_mfma_f32_32x32x16_bf16 v[144:159], v[92:95], v[188:191], v[144:159]\n"
        "ds_read_b64_tr_b16 v[174:175], v13 offset:10752\n"
        "ds_read_b64_tr_b16 v[100:101], v237 offset:5120\n"
        "ds_read_b64_tr_b16 v[102:103], v237 offset:5376\n"
        "v_mfma_f32_32x32x16_bf16 v[192:207], v[108:111], v[92:95], v[192:207]\n"
        "s_add_u32 m0, s70, 0\n"
        "s_mul_i32 s71, s92, 172032\n"
        "buffer_load_dwordx4 v14, s[36:39], s71 offen  lds\n"
        "s_waitcnt lgkmcnt(7)\n"
        "v_mfma_f32_32x32x16_bf16 v[128:143], v[80:83], v[160:163], v[128:143]\n"
        "ds_read_b128 v[88:91], v10 offset:16384\n"
        "ds_read_b64_tr_b16 v[176:177], v12 offset:12288\n"
        "ds_read_b64_tr_b16 v[178:179], v12 offset:12800\n"
        "ds_read_b64_tr_b16 v[180:181], v13 offset:12288\n"
        "v_mfma_f32_32x32x16_bf16 v[144:159], v[80:83], v[164:167], v[144:159]\n"
        "ds_read_b64_tr_b16 v[182:183], v13 offset:12800\n"
        "ds_read_b64_tr_b16 v[104:105], v237 offset:6144\n"
        "ds_read_b64_tr_b16 v[106:107], v237 offset:6400\n"
        "v_mfma_f32_32x32x16_bf16 v[192:207], v[96:99], v[80:83], v[192:207]\n"
        "s_add_u32 m0, s70, 1024\n"
        "s_nop 0\n"
        "buffer_load_dwordx4 v15, s[36:39], s71 offen  lds\n"
        "s_waitcnt lgkmcnt(7)\n"
        "v_mfma_f32_32x32x16_bf16 v[128:143], v[84:87], v[168:171], v[128:143]\n"
        "ds_read_b128 v[92:95], v11 offset:16384\n"
        "ds_read_b64_tr_b16 v[184:185], v12 offset:14336\n"
        "ds_read_b64_tr_b16 v[186:187], v12 offset:14848\n"
        "ds_read_b64_tr_b16 v[188:189], v13 offset:14336\n"
        "v_mfma_f32_32x32x16_bf16 v[144:159], v[84:87], v[172:175], v[144:159]\n"
        "ds_read_b64_tr_b16 v[190:191], v13 offset:14848\n"
        "ds_read_b64_tr_b16 v[108:109], v237 offset:7168\n"
        "ds_read_b64_tr_b16 v[110:111], v237 offset:7424\n"
        "v_mfma_f32_32x32x16_bf16 v[192:207], v[100:103], v[84:87], v[192:207]\n"
        "s_add_u32 m0, s80, 12288\n"
        "s_nop 0\n"
        "buffer_load_dwordx4 v236, s[36:39], s71 offen  lds\n"
        "s_waitcnt lgkmcnt(7)\n"
        "v_mfma_f32_32x32x16_bf16 v[128:143], v[88:91], v[176:179], v[128:143]\n"
        "v_mfma_f32_32x32x16_bf16 v[144:159], v[88:91], v[180:183], v[144:159]\n"
        "v_mfma_f32_32x32x16_bf16 v[192:207], v[104:107], v[88:91], v[192:207]\n"
        "s_waitcnt vmcnt(4)\n"
        "s_waitcnt lgkmcnt(0)\n"
        "s_barrier\n"
        "ds_read_b128 v[80:83], v8 offset:32768\n"
        "ds_read_b64_tr_b16 v[160:161], v12 offset:16384\n"
        "ds_read_b64_tr_b16 v[162:163], v12 offset:16896\n"
        "ds_read_b64_tr_b16 v[164:165], v13 offset:16384\n"
        "ds_read_b64_tr_b16 v[166:167], v13 offset:16896\n"
        "ds_read_b64_tr_b16 v[96:97], v237 offset:8192\n"
        "ds_read_b64_tr_b16 v[98:99], v237 offset:8448\n"
        "v_mfma_f32_32x32x16_bf16 v[128:143], v[92:95], v[184:187], v[128:143]\n"
        "ds_read_b128 v[84:87], v9 offset:32768\n"
        "ds_read_b64_tr_b16 v[168:169], v12 offset:18432\n"
        "ds_read_b64_tr_b16 v[170:171], v12 offset:18944\n"
        "ds_read_b64_tr_b16 v[172:173], v13 offset:18432\n"
        "v_mfma_f32_32x32x16_bf16 v[144:159], v[92:95], v[188:191], v[144:159]\n"
        "ds_read_b64_tr_b16 v[174:175], v13 offset:18944\n"
        "ds_read_b64_tr_b16 v[100:101], v237 offset:9216\n"
        "ds_read_b64_tr_b16 v[102:103], v237 offset:9472\n"
        "v_mfma_f32_32x32x16_bf16 v[192:207], v[108:111], v[92:95], v[192:207]\n"
        "s_add_u32 m0, s70, 8192\n"
        "s_mul_i32 s71, s93, 172032\n"
        "buffer_load_dwordx4 v14, s[36:39], s71 offen  lds\n"
        "s_waitcnt lgkmcnt(7)\n"
        "v_mfma_f32_32x32x16_bf16 v[128:143], v[80:83], v[160:163], v[128:143]\n"
        "ds_read_b128 v[88:91], v10 offset:32768\n"
        "ds_read_b64_tr_b16 v[176:177], v12 offset:20480\n"
        "ds_read_b64_tr_b16 v[178:179], v12 offset:20992\n"
        "ds_read_b64_tr_b16 v[180:181], v13 offset:20480\n"
        "v_mfma_f32_32x32x16_bf16 v[144:159], v[80:83], v[164:167], v[144:159]\n"
        "ds_read_b64_tr_b16 v[182:183], v13 offset:20992\n"
        "ds_read_b64_tr_b16 v[104:105], v237 offset:10240\n"
        "ds_read_b64_tr_b16 v[106:107], v237 offset:10496\n"
        "v_mfma_f32_32x32x16_bf16 v[192:207], v[96:99], v[80:83], v[192:207]\n"
        "s_add_u32 m0, s70, 9216\n"
        "s_nop 0\n"
        "buffer_load_dwordx4 v15, s[36:39], s71 offen  lds\n"
        "s_waitcnt lgkmcnt(7)\n"
        "v_mfma_f32_32x32x16_bf16 v[128:143], v[84:87], v[168:171], v[128:143]\n"
        "ds_read_b128 v[92:95], v11 offset:32768\n"
        "ds_read_b64_tr_b16 v[184:185], v12 offset:22528\n"
        "ds_read_b64_tr_b16 v[186:187], v12 offset:23040\n"
        "ds_read_b64_tr_b16 v[188:189], v13 offset:22528\n"
        "v_mfma_f32_32x32x16_bf16 v[144:159], v[84:87], v[172:175], v[144:159]\n"
        "ds_read_b64_tr_b16 v[190:191], v13 offset:23040\n"
        "ds_read_b64_tr_b16 v[108:109], v237 offset:11264\n"
        "ds_read_b64_tr_b16 v[110:111], v237 offset:11520\n"
        "v_mfma_f32_32x32x16_bf16 v[192:207], v[100:103], v[84:87], v[192:207]\n"
        "s_add_u32 m0, s80, 16384\n"
        "s_nop 0\n"
        "buffer_load_dwordx4 v236, s[36:39], s71 offen  lds\n"
        "s_waitcnt lgkmcnt(7)\n"
        "v_mfma_f32_32x32x16_bf16 v[128:143], v[88:91], v[176:179], v[128:143]\n"
        "v_mfma_f32_32x32x16_bf16 v[144:159], v[88:91], v[180:183], v[144:159]\n"
        "v_mfma_f32_32x32x16_bf16 v[192:207], v[104:107], v[88:91], v[192:207]\n"
        "s_waitcnt vmcnt(3)\n"
        "s_waitcnt lgkmcnt(0)\n"
        "s_barrier\n"
        "ds_read_b128 v[80:83], v8 offset:0\n"
        "ds_read_b64_tr_b16 v[160:161], v12 offset:0\n"
        "ds_read_b64_tr_b16 v[162:163], v12 offset:512\n"
        "ds_read_b64_tr_b16 v[164:165], v13 offset:0\n"
        "ds_read_b64_tr_b16 v[166:167], v13 offset:512\n"
        "ds_read_b64_tr_b16 v[96:97], v237 offset:12288\n"
        "ds_read_b64_tr_b16 v[98:99], v237 offset:12544\n"
        "v_mfma_f32_32x32x16_bf16 v[128:143], v[92:95], v[184:187], v[128:143]\n"
        "ds_read_b128 v[84:87], v9 offset:0\n"
        "ds_read_b64_tr_b16 v[168:169], v12 offset:2048\n"
        "ds_read_b64_tr_b16 v[170:171], v12 offset:2560\n"
        "ds_read_b64_tr_b16 v[172:173], v13 offset:2048\n"
        "v_mfma_f32_32x32x16_bf16 v[144:159], v[92:95], v[188:191], v[144:159]\n"
        "ds_read_b64_tr_b16 v[174:175], v13 offset:2560\n"
        "ds_read_b64_tr_b16 v[100:101], v237 offset:13312\n"
        "ds_read_b64_tr_b16 v[102:103], v237 offset:13568\n"
        "v_mfma_f32_32x32x16_bf16 v[192:207], v[108:111], v[92:95], v[192:207]\n"
        "s_add_u32 m0, s70, 16384\n"
        "s_mul_i32 s71, s94, 172032\n"
        "buffer_load_dwordx4 v14, s[36:39], s71 offen  lds\n"
        "s_waitcnt lgkmcnt(7)\n"
        "v_mfma_f32_32x32x16_bf16 v[128:143], v[80:83], v[160:163], v[128:143]\n"
        "ds_read_b128 v[88:91], v10 offset:0\n"
        "ds_read_b64_tr_b16 v[176:177], v12 offset:4096\n"
        "ds_read_b64_tr_b16 v[178:179], v12 offset:4608\n"
        "ds_read_b64_tr_b16 v[180:181], v13 offset:4096\n"
        "v_mfma_f32_32x32x16_bf16 v[144:159], v[80:83], v[164:167], v[144:159]\n"
        "ds_read_b64_tr_b16 v[182:183], v13 offset:4608\n"
        "ds_read_b64_tr_b16 v[104:105], v237 offset:14336\n"
        "ds_read_b64_tr_b16 v[106:107], v237 offset:14592\n"
        "v_mfma_f32_32x32x16_bf16 v[192:207], v[96:99], v[80:83], v[192:207]\n"
        "s_add_u32 m0, s70, 17408\n"
        "s_nop 0\n"
        "buffer_load_dwordx4 v15, s[36:39], s71 offen  lds\n"
        "s_waitcnt lgkmcnt(7)\n"
        "v_mfma_f32_32x32x16_bf16 v[128:143], v[84:87], v[168:171], v[128:143]\n"
        "ds_read_b128 v[92:95], v11 offset:0\n"
        "ds_read_b64_tr_b16 v[184:185], v12 offset:6144\n"
        "ds_read_b64_tr_b16 v[186:187], v12 offset:6656\n"
        "ds_read_b64_tr_b16 v[188:189], v13 offset:6144\n"
        "v_mfma_f32_32x32x16_bf16 v[144:159], v[84:87], v[172:175], v[144:159]\n"
        "ds_read_b64_tr_b16 v[190:191], v13 offset:6656\n"
        "ds_read_b64_tr_b16 v[108:109], v237 offset:15360\n"
        "ds_read_b64_tr_b16 v[110:111], v237 offset:15616\n"
        "v_mfma_f32_32x32x16_bf16 v[192:207], v[100:103], v[84:87], v[192:207]\n"
        "s_add_u32 m0, s80, 20480\n"
        "s_nop 0\n"
        "buffer_load_dwordx4 v236, s[36:39], s71 offen  lds\n"
        "s_waitcnt lgkmcnt(7)\n"
        "v_mfma_f32_32x32x16_bf16 v[128:143], v[88:91], v[176:179], v[128:143]\n"
        "v_mfma_f32_32x32x16_bf16 v[144:159], v[88:91], v[180:183], v[144:159]\n"
        "v_mfma_f32_32x32x16_bf16 v[192:207], v[104:107], v[88:91], v[192:207]\n"
        "s_waitcnt vmcnt(3)\n"
        "s_waitcnt lgkmcnt(0)\n"
        "s_barrier\n"
        "ds_read_b128 v[80:83], v8 offset:16384\n"
        "ds_read_b64_tr_b16 v[160:161], v12 offset:8192\n"
        "ds_read_b64_tr_b16 v[162:163], v12 offset:8704\n"
        "ds_read_b64_tr_b16 v[164:165], v13 offset:8192\n"
        "ds_read_b64_tr_b16 v[166:167], v13 offset:8704\n"
        "ds_read_b64_tr_b16 v[96:97], v237 offset:16384\n"
        "ds_read_b64_tr_b16 v[98:99], v237 offset:16640\n"
        "v_mfma_f32_32x32x16_bf16 v[128:143], v[92:95], v[184:187], v[128:143]\n"
        "ds_read_b128 v[84:87], v9 offset:16384\n"
        "ds_read_b64_tr_b16 v[168:169], v12 offset:10240\n"
        "ds_read_b64_tr_b16 v[170:171], v12 offset:10752\n"
        "ds_read_b64_tr_b16 v[172:173], v13 offset:10240\n"
        "v_mfma_f32_32x32x16_bf16 v[144:159], v[92:95], v[188:191], v[144:159]\n"
        "ds_read_b64_tr_b16 v[174:175], v13 offset:10752\n"
        "ds_read_b64_tr_b16 v[100:101], v237 offset:17408\n"
        "ds_read_b64_tr_b16 v[102:103], v237 offset:17664\n"
        "v_mfma_f32_32x32x16_bf16 v[192:207], v[108:111], v[92:95], v[192:207]\n"
        "s_add_u32 m0, s70, 0\n"
        "s_mul_i32 s71, s95, 172032\n"
        "buffer_load_dwordx4 v14, s[36:39], s71 offen  lds\n"
        "s_waitcnt lgkmcnt(7)\n"
        "v_mfma_f32_32x32x16_bf16 v[128:143], v[80:83], v[160:163], v[128:143]\n"
        "ds_read_b128 v[88:91], v10 offset:16384\n"
        "ds_read_b64_tr_b16 v[176:177], v12 offset:12288\n"
        "ds_read_b64_tr_b16 v[178:179], v12 offset:12800\n"
        "ds_read_b64_tr_b16 v[180:181], v13 offset:12288\n"
        "v_mfma_f32_32x32x16_bf16 v[144:159], v[80:83], v[164:167], v[144:159]\n"
        "ds_read_b64_tr_b16 v[182:183], v13 offset:12800\n"
        "ds_read_b64_tr_b16 v[104:105], v237 offset:18432\n"
        "ds_read_b64_tr_b16 v[106:107], v237 offset:18688\n"
        "v_mfma_f32_32x32x16_bf16 v[192:207], v[96:99], v[80:83], v[192:207]\n"
        "s_add_u32 m0, s70, 1024\n"
        "s_nop 0\n"
        "buffer_load_dwordx4 v15, s[36:39], s71 offen  lds\n"
        "s_waitcnt lgkmcnt(7)\n"
        "v_mfma_f32_32x32x16_bf16 v[128:143], v[84:87], v[168:171], v[128:143]\n"
        "ds_read_b128 v[92:95], v11 offset:16384\n"
        "ds_read_b64_tr_b16 v[184:185], v12 offset:14336\n"
        "ds_read_b64_tr_b16 v[186:187], v12 offset:14848\n"
        "ds_read_b64_tr_b16 v[188:189], v13 offset:14336\n"
        "v_mfma_f32_32x32x16_bf16 v[144:159], v[84:87], v[172:175], v[144:159]\n"
        "ds_read_b64_tr_b16 v[190:191], v13 offset:14848\n"
        "ds_read_b64_tr_b16 v[108:109], v237 offset:19456\n"
        "ds_read_b64_tr_b16 v[110:111], v237 offset:19712\n"
        "v_mfma_f32_32x32x16_bf16 v[192:207], v[100:103], v[84:87], v[192:207]\n"
        "s_add_u32 m0, s80, 24576\n"
        "s_nop 0\n"
        "buffer_load_dwordx4 v236, s[36:39], s71 offen  lds\n"
        "s_waitcnt lgkmcnt(7)\n"
        "v_mfma_f32_32x32x16_bf16 v[128:143], v[88:91], v[176:179], v[128:143]\n"
        "v_mfma_f32_32x32x16_bf16 v[144:159], v[88:91], v[180:183], v[144:159]\n"
        "v_mfma_f32_32x32x16_bf16 v[192:207], v[104:107], v[88:91], v[192:207]\n"
        "s_waitcnt vmcnt(3)\n"
        "s_waitcnt lgkmcnt(0)\n"
        "s_barrier\n"
        "ds_read_b128 v[80:83], v8 offset:32768\n"
        "ds_read_b64_tr_b16 v[160:161], v12 offset:16384\n"
        "ds_read_b64_tr_b16 v[162:163], v12 offset:16896\n"
        "ds_read_b64_tr_b16 v[164:165], v13 offset:16384\n"
        "ds_read_b64_tr_b16 v[166:167], v13 offset:16896\n"
        "ds_read_b64_tr_b16 v[96:97], v237 offset:20480\n"
        "ds_read_b64_tr_b16 v[98:99], v237 offset:20736\n"
        "v_mfma_f32_32x32x16_bf16 v[128:143], v[92:95], v[184:187], v[128:143]\n"
        "ds_read_b128 v[84:87], v9 offset:32768\n"
        "ds_read_b64_tr_b16 v[168:169], v12 offset:18432\n"
        "ds_read_b64_tr_b16 v[170:171], v12 offset:18944\n"
        "v_mfma_f32_32x32x16_bf16 v[144:159], v[92:95], v[188:191], v[144:159]\n"
        "ds_read_b64_tr_b16 v[172:173], v13 offset:18432\n"
        "ds_read_b64_tr_b16 v[174:175], v13 offset:18944\n"
        "v_mfma_f32_32x32x16_bf16 v[192:207], v[108:111], v[92:95], v[192:207]\n"
        "ds_read_b64_tr_b16 v[100:101], v237 offset:21504\n"
        "ds_read_b64_tr_b16 v[102:103], v237 offset:21760\n"
        "s_waitcnt lgkmcnt(7)\n"
        "v_mfma_f32_32x32x16_bf16 v[128:143], v[80:83], v[160:163], v[128:143]\n"
        "ds_read_b128 v[88:91], v10 offset:32768\n"
        "ds_read_b64_tr_b16 v[176:177], v12 offset:20480\n"
        "ds_read_b64_tr_b16 v[178:179], v12 offset:20992\n"
        "v_mfma_f32_32x32x16_bf16 v[144:159], v[80:83], v[164:167], v[144:159]\n"
        "ds_read_b64_tr_b16 v[180:181], v13 offset:20480\n"
        "ds_read_b64_tr_b16 v[182:183], v13 offset:20992\n"
        "v_mfma_f32_32x32x16_bf16 v[192:207], v[96:99], v[80:83], v[192:207]\n"
        "ds_read_b64_tr_b16 v[104:105], v237 offset:22528\n"
        "ds_read_b64_tr_b16 v[106:107], v237 offset:22784\n"
        "s_waitcnt lgkmcnt(7)\n"
        "v_mfma_f32_32x32x16_bf16 v[128:143], v[84:87], v[168:171], v[128:143]\n"
        "ds_read_b128 v[92:95], v11 offset:32768\n"
        "ds_read_b64_tr_b16 v[184:185], v12 offset:22528\n"
        "ds_read_b64_tr_b16 v[186:187], v12 offset:23040\n"
        "v_mfma_f32_32x32x16_bf16 v[144:159], v[84:87], v[172:175], v[144:159]\n"
        "ds_read_b64_tr_b16 v[188:189], v13 offset:22528\n"
        "ds_read_b64_tr_b16 v[190:191], v13 offset:23040\n"
        "v_mfma_f32_32x32x16_bf16 v[192:207], v[100:103], v[84:87], v[192:207]\n"
        "ds_read_b64_tr_b16 v[108:109], v237 offset:23552\n"
        "ds_read_b64_tr_b16 v[110:111], v237 offset:23808\n"
        "s_waitcnt lgkmcnt(7)\n"
        "v_mfma_f32_32x32x16_bf16 v[128:143], v[88:91], v[176:179], v[128:143]\n"
        "v_mfma_f32_32x32x16_bf16 v[144:159], v[88:91], v[180:183], v[144:159]\n"
        "v_mfma_f32_32x32x16_bf16 v[192:207], v[104:107], v[88:91], v[192:207]\n"
        "s_waitcnt vmcnt(0)\n"
        "s_waitcnt lgkmcnt(0)\n"
        "s_barrier\n"
        "ds_read_b128 v[80:83], v8 offset:0\n"
        "ds_read_b64_tr_b16 v[160:161], v12 offset:0\n"
        "ds_read_b64_tr_b16 v[162:163], v12 offset:512\n"
        "ds_read_b64_tr_b16 v[164:165], v13 offset:0\n"
        "ds_read_b64_tr_b16 v[166:167], v13 offset:512\n"
        "ds_read_b64_tr_b16 v[96:97], v237 offset:24576\n"
        "ds_read_b64_tr_b16 v[98:99], v237 offset:24832\n"
        "v_mfma_f32_32x32x16_bf16 v[128:143], v[92:95], v[184:187], v[128:143]\n"
        "ds_read_b128 v[84:87], v9 offset:0\n"
        "ds_read_b64_tr_b16 v[168:169], v12 offset:2048\n"
        "ds_read_b64_tr_b16 v[170:171], v12 offset:2560\n"
        "v_mfma_f32_32x32x16_bf16 v[144:159], v[92:95], v[188:191], v[144:159]\n"
        "ds_read_b64_tr_b16 v[172:173], v13 offset:2048\n"
        "ds_read_b64_tr_b16 v[174:175], v13 offset:2560\n"
        "v_mfma_f32_32x32x16_bf16 v[192:207], v[108:111], v[92:95], v[192:207]\n"
        "ds_read_b64_tr_b16 v[100:101], v237 offset:25600\n"
        "ds_read_b64_tr_b16 v[102:103], v237 offset:25856\n"
        "s_waitcnt lgkmcnt(7)\n"
        "v_mfma_f32_32x32x16_bf16 v[128:143], v[80:83], v[160:163], v[128:143]\n"
        "ds_read_b128 v[88:91], v10 offset:0\n"
        "ds_read_b64_tr_b16 v[176:177], v12 offset:4096\n"
        "ds_read_b64_tr_b16 v[178:179], v12 offset:4608\n"
        "v_mfma_f32_32x32x16_bf16 v[144:159], v[80:83], v[164:167], v[144:159]\n"
        "ds_read_b64_tr_b16 v[180:181], v13 offset:4096\n"
        "ds_read_b64_tr_b16 v[182:183], v13 offset:4608\n"
        "v_mfma_f32_32x32x16_bf16 v[192:207], v[96:99], v[80:83], v[192:207]\n"
        "ds_read_b64_tr_b16 v[104:105], v237 offset:26624\n"
        "ds_read_b64_tr_b16 v[106:107], v237 offset:26880\n"
        "s_waitcnt lgkmcnt(7)\n"
        "v_mfma_f32_32x32x16_bf16 v[128:143], v[84:87], v[168:171], v[128:143]\n"
        "ds_read_b128 v[92:95], v11 offset:0\n"
        "ds_read_b64_tr_b16 v[184:185], v12 offset:6144\n"
        "ds_read_b64_tr_b16 v[186:187], v12 offset:6656\n"
        "v_mfma_f32_32x32x16_bf16 v[144:159], v[84:87], v[172:175], v[144:159]\n"
        "ds_read_b64_tr_b16 v[188:189], v13 offset:6144\n"
        "ds_read_b64_tr_b16 v[190:191], v13 offset:6656\n"
        "v_mfma_f32_32x32x16_bf16 v[192:207], v[100:103], v[84:87], v[192:207]\n"
        "ds_read_b64_tr_b16 v[108:109], v237 offset:27648\n"
        "ds_read_b64_tr_b16 v[110:111], v237 offset:27904\n"
        "s_waitcnt lgkmcnt(7)\n"
        "v_mfma_f32_32x32x16_bf16 v[128:143], v[88:91], v[176:179], v[128:143]\n"
        "v_mfma_f32_32x32x16_bf16 v[144:159], v[88:91], v[180:183], v[144:159]\n"
        "v_mfma_f32_32x32x16_bf16 v[192:207], v[104:107], v[88:91], v[192:207]\n"
        "v_and_b32 v1, 31, v0\n"
        "v_lshlrev_b32 v1, 4, v1\n"
        "s_lshl_b32 s18, s10, 9\n"
        "s_add_u32 s18, s18, 160768\n"
        "v_add_u32 v1, s18, v1\n"
        "v_add_u32 v3, 106496, v240\n"
        "v_lshrrev_b32 v2, 5, v0\n"
        "v_lshlrev_b32 v2, 4, v2\n"
        "s_lshl_b32 s18, s10, 7\n"
        "s_add_u32 s18, s18, 160000\n"
        "v_add_u32 v2, s18, v2\n"
        "s_waitcnt lgkmcnt(0)\n"
        "s_barrier\n"
        "ds_read_b128 v[208:211], v3\n"
        "ds_read_b128 v[212:215], v3 offset:1024\n"
        "ds_read_b128 v[224:227], v1\n"
        "ds_read_b128 v[216:219], v3 offset:2048\n"
        "ds_read_b128 v[160:163], v2 offset:0\n"
        "ds_read_b128 v[164:167], v2 offset:32\n"
        "ds_read_b128 v[168:171], v2 offset:64\n"
        "ds_read_b128 v[172:175], v2 offset:96\n"
        "v_mfma_f32_32x32x16_bf16 v[128:143], v[92:95], v[184:187], v[128:143]\n"
        "v_mfma_f32_32x32x16_bf16 v[144:159], v[92:95], v[188:191], v[144:159]\n"
        "v_mfma_f32_32x32x16_bf16 v[192:207], v[108:111], v[92:95], v[192:207]\n"
        "s_waitcnt lgkmcnt(6)\n"
        "v_pk_add_f32 v[208:209], v[208:209], v[210:211]\n"
        "v_add_f32 v208, v208, v209\n"
        "v_pk_add_f32 v[212:213], v[212:213], v[214:215]\n"
        "v_add_f32 v212, v212, v213\n"
        "v_cvt_pk_bf16_f32 v3, v208, v208\n"
        "v_and_b32 v4, 0xffff0000, v3\n"
        "v_sub_f32 v4, v208, v4\n"
        "v_cvt_pk_bf16_f32 v120, v208, v4\n"
        "v_and_b32 v121, 0xffff, v3\n"
        "v_cvt_pk_bf16_f32 v5, v212, v212\n"
        "v_and_b32 v6, 0xffff0000, v5\n"
        "v_sub_f32 v6, v212, v6\n"
        "v_cvt_pk_bf16_f32 v124, v212, v6\n"
        "v_and_b32 v125, 0xffff, v5\n"
        "s_waitcnt lgkmcnt(0)\n"
        "v_mov_b32 v234, v227\n"
        "v_mov_b32 v227, 0\n"
        "s_nop 1\n"
        "v_mfma_f32_32x32x16_bf16 v[128:143], v[224:227], v[120:123], v[128:143]\n"
        "v_mfma_f32_32x32x16_bf16 v[144:159], v[224:227], v[124:127], v[144:159]\n"
        "v_pk_add_f32 v[216:217], v[216:217], v[218:219]\n"
        "v_add_f32 v216, v216, v217\n"
        "v_cvt_pk_bf16_f32 v3, v216, v216\n"
        "v_and_b32 v4, 0xffff0000, v3\n"
        "v_sub_f32 v4, v216, v4\n"
        "v_cvt_pk_bf16_f32 v228, v216, v4\n"
        "v_and_b32 v229, 0xffff, v3\n"
        "s_lshl_b32 s18, s72, 11\n"
        "s_add_u32 s18, s18, 2097152\n"
        "s_nop 1\n"
        "v_mfma_f32_32x32x16_bf16 v[192:207], v[228:231], v[224:227], v[192:207]\n"
        "v_pk_mul_f32 v[128:129], v[128:129], v[160:161]\n"
        "v_pk_mul_f32 v[130:131], v[130:131], v[162:163]\n"
        "v_pk_mul_f32 v[132:133], v[132:133], v[164:165]\n"
        "v_pk_mul_f32 v[134:135], v[134:135], v[166:167]\n"
        "v_cvt_pk_bf16_f32 v176, v128, v129\n"
        "v_cvt_pk_bf16_f32 v177, v130, v131\n"
        "v_cvt_pk_bf16_f32 v178, v132, v133\n"
        "v_cvt_pk_bf16_f32 v179, v134, v135\n"
        "buffer_store_dwordx4 v[176:179], v232, s[64:67], s18 offen offset:0 sc1\n"
        "v_pk_mul_f32 v[136:137], v[136:137], v[168:169]\n"
        "v_pk_mul_f32 v[138:139], v[138:139], v[170:171]\n"
        "v_pk_mul_f32 v[140:141], v[140:141], v[172:173]\n"
        "v_pk_mul_f32 v[142:143], v[142:143], v[174:175]\n"
        "v_cvt_pk_bf16_f32 v180, v136, v137\n"
        "v_cvt_pk_bf16_f32 v181, v138, v139\n"
        "v_cvt_pk_bf16_f32 v182, v140, v141\n"
        "v_cvt_pk_bf16_f32 v183, v142, v143\n"
        "buffer_store_dwordx4 v[180:183], v232, s[64:67], s18 offen offset:1024 sc1\n"
        "v_pk_mul_f32 v[144:145], v[144:145], v[160:161]\n"
        "v_pk_mul_f32 v[146:147], v[146:147], v[162:163]\n"
        "v_pk_mul_f32 v[148:149], v[148:149], v[164:165]\n"
        "v_pk_mul_f32 v[150:151], v[150:151], v[166:167]\n"
        "v_cvt_pk_bf16_f32 v184, v144, v145\n"
        "v_cvt_pk_bf16_f32 v185, v146, v147\n"
        "v_cvt_pk_bf16_f32 v186, v148, v149\n"
        "v_cvt_pk_bf16_f32 v187, v150, v151\n"
        "buffer_store_dwordx4 v[184:187], v233, s[64:67], s18 offen offset:0 sc1\n"
        "v_pk_mul_f32 v[152:153], v[152:153], v[168:169]\n"
        "v_pk_mul_f32 v[154:155], v[154:155], v[170:171]\n"
        "v_pk_mul_f32 v[156:157], v[156:157], v[172:173]\n"
        "v_pk_mul_f32 v[158:159], v[158:159], v[174:175]\n"
        "v_cvt_pk_bf16_f32 v188, v152, v153\n"
        "v_cvt_pk_bf16_f32 v189, v154, v155\n"
        "v_cvt_pk_bf16_f32 v190, v156, v157\n"
        "v_cvt_pk_bf16_f32 v191, v158, v159\n"
        "buffer_store_dwordx4 v[188:191], v233, s[64:67], s18 offen offset:1024 sc1\n"
        "s_branch .LA_q\n"
        ".LA_idle:\n"
        "s_waitcnt vmcnt(2)\n"
        "s_barrier\n"
        "ds_read_b128 v[80:83], v8 offset:0\n"
        "ds_read_b64_tr_b16 v[96:97], v237 offset:0\n"
        "ds_read_b64_tr_b16 v[98:99], v237 offset:256\n"
        "ds_read_b128 v[84:87], v9 offset:0\n"
        "ds_read_b64_tr_b16 v[100:101], v237 offset:1024\n"
        "ds_read_b64_tr_b16 v[102:103], v237 offset:1280\n"
        "s_waitcnt lgkmcnt(3)\n"
        "v_mfma_f32_32x32x16_bf16 v[192:207], v[96:99], v[80:83], 0\n"
        "ds_read_b128 v[88:91], v10 offset:0\n"
        "ds_read_b64_tr_b16 v[104:105], v237 offset:2048\n"
        "ds_read_b64_tr_b16 v[106:107], v237 offset:2304\n"
        "s_waitcnt lgkmcnt(3)\n"
        "v_mfma_f32_32x32x16_bf16 v[192:207], v[100:103], v[84:87], v[192:207]\n"
        "ds_read_b128 v[92:95], v11 offset:0\n"
        "ds_read_b64_tr_b16 v[108:109], v237 offset:3072\n"
        "ds_read_b64_tr_b16 v[110:111], v237 offset:3328\n"
        "s_waitcnt lgkmcnt(3)\n"
        "v_mfma_f32_32x32x16_bf16 v[192:207], v[104:107], v[88:91], v[192:207]\n"
        "s_waitcnt vmcnt(1)\n"
        "s_waitcnt lgkmcnt(0)\n"
        "s_barrier\n"
        "ds_read_b128 v[80:83], v8 offset:16384\n"
        "ds_read_b64_tr_b16 v[96:97], v237 offset:4096\n"
        "ds_read_b64_tr_b16 v[98:99], v237 offset:4352\n"
        "v_mfma_f32_32x32x16_bf16 v[192:207], v[108:111], v[92:95], v[192:207]\n"
        "ds_read_b128 v[84:87], v9 offset:16384\n"
        "ds_read_b64_tr_b16 v[100:101], v237 offset:5120\n"
        "ds_read_b64_tr_b16 v[102:103], v237 offset:5376\n"
        "s_add_u32 m0, s80, 12288\n"
        "s_mul_i32 s71, s92, 172032\n"
        "buffer_load_dwordx4 v236, s[36:39], s71 offen  lds\n"
        "s_waitcnt lgkmcnt(3)\n"
        "v_mfma_f32_32x32x16_bf16 v[192:207], v[96:99], v[80:83], v[192:207]\n"
        "ds_read_b128 v[88:91], v10 offset:16384\n"
        "ds_read_b64_tr_b16 v[104:105], v237 offset:6144\n"
        "ds_read_b64_tr_b16 v[106:107], v237 offset:6400\n"
        "s_waitcnt lgkmcnt(3)\n"
        "v_mfma_f32_32x32x16_bf16 v[192:207], v[100:103], v[84:87], v[192:207]\n"
        "ds_read_b128 v[92:95], v11 offset:16384\n"
        "ds_read_b64_tr_b16 v[108:109], v237 offset:7168\n"
        "ds_read_b64_tr_b16 v[110:111], v237 offset:7424\n"
        "s_waitcnt lgkmcnt(3)\n"
        "v_mfma_f32_32x32x16_bf16 v[192:207], v[104:107], v[88:91], v[192:207]\n"
        "s_waitcnt vmcnt(1)\n"
        "s_waitcnt lgkmcnt(0)\n"
        "s_barrier\n"
        "ds_read_b128 v[80:83], v8 offset:32768\n"
        "ds_read_b64_tr_b16 v[96:97], v237 offset:8192\n"
        "ds_read_b64_tr_b16 v[98:99], v237 offset:8448\n"
        "v_mfma_f32_32x32x16_bf16 v[192:207], v[108:111], v[92:95], v[192:207]\n"
        "ds_read_b128 v[84:87], v9 offset:32768\n"
        "ds_read_b64_tr_b16 v[100:101], v237 offset:9216\n"
        "ds_read_b64_tr_b16 v[102:103], v237 offset:9472\n"
        "s_add_u32 m0, s80, 16384\n"
        "s_mul_i32 s71, s93, 172032\n"
        "buffer_load_dwordx4 v236, s[36:39], s71 offen  lds\n"
        "s_waitcnt lgkmcnt(3)\n"
        "v_mfma_f32_32x32x16_bf16 v[192:207], v[96:99], v[80:83], v[192:207]\n"
        "ds_read_b128 v[88:91], v10 offset:32768\n"
        "ds_read_b64_tr_b16 v[104:105], v237 offset:10240\n"
        "ds_read_b64_tr_b16 v[106:107], v237 offset:10496\n"
        "s_waitcnt lgkmcnt(3)\n"
        "v_mfma_f32_32x32x16_bf16 v[192:207], v[100:103], v[84:87], v[192:207]\n"
        "ds_read_b128 v[92:95], v11 offset:32768\n"
        "ds_read_b64_tr_b16 v[108:109], v237 offset:11264\n"
        "ds_read_b64_tr_b16 v[110:111], v237 offset:11520\n"
        "s_waitcnt lgkmcnt(3)\n"
        "v_mfma_f32_32x32x16_bf16 v[192:207], v[104:107], v[88:91], v[192:207]\n"
        "s_waitcnt vmcnt(1)\n"
        "s_waitcnt lgkmcnt(0)\n"
        "s_barrier\n"
        "ds_read_b128 v[80:83], v8 offset:0\n"
        "ds_read_b64_tr_b16 v[96:97], v237 offset:12288\n"
        "ds_read_b64_tr_b16 v[98:99], v237 offset:12544\n"
        "v_mfma_f32_32x32x16_bf16 v[192:207], v[108:111], v[92:95], v[192:207]\n"
        "ds_read_b128 v[84:87], v9 offset:0\n"
        "ds_read_b64_tr_b16 v[100:101], v237 offset:13312\n"
        "ds_read_b64_tr_b16 v[102:103], v237 offset:13568\n"
        "s_add_u32 m0, s80, 20480\n"
        "s_mul_i32 s71, s94, 172032\n"
        "buffer_load_dwordx4 v236, s[36:39], s71 offen  lds\n"
        "s_waitcnt lgkmcnt(3)\n"
        "v_mfma_f32_32x32x16_bf16 v[192:207], v[96:99], v[80:83], v[192:207]\n"
        "ds_read_b128 v[88:91], v10 offset:0\n"
        "ds_read_b64_tr_b16 v[104:105], v237 offset:14336\n"
        "ds_read_b64_tr_b16 v[106:107], v237 offset:14592\n"
        "s_waitcnt lgkmcnt(3)\n"
        "v_mfma_f32_32x32x16_bf16 v[192:207], v[100:103], v[84:87], v[192:207]\n"
        "ds_read_b128 v[92:95], v11 offset:0\n"
        "ds_read_b64_tr_b16 v[108:109], v237 offset:15360\n"
        "ds_read_b64_tr_b16 v[110:111], v237 offset:15616\n"
        "s_waitcnt lgkmcnt(3)\n"
        "v_mfma_f32_32x32x16_bf16 v[192:207], v[104:107], v[88:91], v[192:207]\n"
        "s_waitcnt vmcnt(1)\n"
        "s_waitcnt lgkmcnt(0)\n"
        "s_barrier\n"
        "ds_read_b128 v[80:83], v8 offset:16384\n"
        "ds_read_b64_tr_b16 v[96:97], v237 offset:16384\n"
        "ds_read_b64_tr_b16 v[98:99], v237 offset:16640\n"
        "v_mfma_f32_32x32x16_bf16 v[192:207], v[108:111], v[92:95], v[192:207]\n"
        "ds_read_b128 v[84:87], v9 offset:16384\n"
        "ds_read_b64_tr_b16 v[100:101], v237 offset:17408\n"
        "ds_read_b64_tr_b16 v[102:103], v237 offset:17664\n"
        "s_add_u32 m0, s80, 24576\n"
        "s_mul_i32 s71, s95, 172032\n"
        "buffer_load_dwordx4 v236, s[36:39], s71 offen  lds\n"
        "s_waitcnt lgkmcnt(3)\n"
        "v_mfma_f32_32x32x16_bf16 v[192:207], v[96:99], v[80:83], v[192:207]\n"
        "ds_read_b128 v[88:91], v10 offset:16384\n"
        "ds_read_b64_tr_b16 v[104:105], v237 offset:18432\n"
        "ds_read_b64_tr_b16 v[106:107], v237 offset:18688\n"
        "s_waitcnt lgkmcnt(3)\n"
        "v_mfma_f32_32x32x16_bf16 v[192:207], v[100:103], v[84:87], v[192:207]\n"
        "ds_read_b128 v[92:95], v11 offset:16384\n"
        "ds_read_b64_tr_b16 v[108:109], v237 offset:19456\n"
        "ds_read_b64_tr_b16 v[110:111], v237 offset:19712\n"
        "s_waitcnt lgkmcnt(3)\n"
        "v_mfma_f32_32x32x16_bf16 v[192:207], v[104:107], v[88:91], v[192:207]\n"
        "s_waitcnt vmcnt(1)\n"
        "s_waitcnt lgkmcnt(0)\n"
        "s_barrier\n"
        "ds_read_b128 v[80:83], v8 offset:32768\n"
        "ds_read_b64_tr_b16 v[96:97], v237 offset:20480\n"
        "ds_read_b64_tr_b16 v[98:99], v237 offset:20736\n"
        "v_mfma_f32_32x32x16_bf16 v[192:207], v[108:111], v[92:95], v[192:207]\n"
        "ds_read_b128 v[84:87], v9 offset:32768\n"
        "ds_read_b64_tr_b16 v[100:101], v237 offset:21504\n"
        "ds_read_b64_tr_b16 v[102:103], v237 offset:21760\n"
        "s_waitcnt lgkmcnt(3)\n"
        "v_mfma_f32_32x32x16_bf16 v[192:207], v[96:99], v[80:83], v[192:207]\n"
        "ds_read_b128 v[88:91], v10 offset:32768\n"
        "ds_read_b64_tr_b16 v[104:105], v237 offset:22528\n"
        "ds_read_b64_tr_b16 v[106:107], v237 offset:22784\n"
        "s_waitcnt lgkmcnt(3)\n"
        "v_mfma_f32_32x32x16_bf16 v[192:207], v[100:103], v[84:87], v[192:207]\n"
        "ds_read_b128 v[92:95], v11 offset:32768\n"
        "ds_read_b64_tr_b16 v[108:109], v237 offset:23552\n"
        "ds_read_b64_tr_b16 v[110:111], v237 offset:23808\n"
        "s_waitcnt lgkmcnt(3)\n"
        "v_mfma_f32_32x32x16_bf16 v[192:207], v[104:107], v[88:91], v[192:207]\n"
        "s_waitcnt vmcnt(0)\n"
        "s_waitcnt lgkmcnt(0)\n"
        "s_barrier\n"
        "ds_read_b128 v[80:83], v8 offset:0\n"
        "ds_read_b64_tr_b16 v[96:97], v237 offset:24576\n"
        "ds_read_b64_tr_b16 v[98:99], v237 offset:24832\n"
        "v_mfma_f32_32x32x16_bf16 v[192:207], v[108:111], v[92:95], v[192:207]\n"
        "ds_read_b128 v[84:87], v9 offset:0\n"
        "ds_read_b64_tr_b16 v[100:101], v237 offset:25600\n"
        "ds_read_b64_tr_b16 v[102:103], v237 offset:25856\n"
        "s_waitcnt lgkmcnt(3)\n"
        "v_mfma_f32_32x32x16_bf16 v[192:207], v[96:99], v[80:83], v[192:207]\n"
        "ds_read_b128 v[88:91], v10 offset:0\n"
        "ds_read_b64_tr_b16 v[104:105], v237 offset:26624\n"
        "ds_read_b64_tr_b16 v[106:107], v237 offset:26880\n"
        "s_waitcnt lgkmcnt(3)\n"
        "v_mfma_f32_32x32x16_bf16 v[192:207], v[100:103], v[84:87], v[192:207]\n"
        "ds_read_b128 v[92:95], v11 offset:0\n"
        "ds_read_b64_tr_b16 v[108:109], v237 offset:27648\n"
        "ds_read_b64_tr_b16 v[110:111], v237 offset:27904\n"
        "s_waitcnt lgkmcnt(3)\n"
        "v_mfma_f32_32x32x16_bf16 v[192:207], v[104:107], v[88:91], v[192:207]\n"
        "v_and_b32 v1, 31, v0\n"
        "v_lshlrev_b32 v1, 4, v1\n"
        "s_lshl_b32 s18, s10, 9\n"
        "s_add_u32 s18, s18, 160768\n"
        "v_add_u32 v1, s18, v1\n"
        "v_add_u32 v3, 106496, v240\n"
        "s_waitcnt lgkmcnt(0)\n"
        "s_barrier\n"
        "ds_read_b128 v[224:227], v1\n"
        "ds_read_b128 v[216:219], v3 offset:2048\n"
        "v_mfma_f32_32x32x16_bf16 v[192:207], v[108:111], v[92:95], v[192:207]\n"
        "s_waitcnt lgkmcnt(0)\n"
        "v_mov_b32 v234, v227\n"
        "v_mov_b32 v227, 0\n"
        "v_pk_add_f32 v[216:217], v[216:217], v[218:219]\n"
        "v_add_f32 v216, v216, v217\n"
        "v_cvt_pk_bf16_f32 v3, v216, v216\n"
        "v_and_b32 v4, 0xffff0000, v3\n"
        "v_sub_f32 v4, v216, v4\n"
        "v_cvt_pk_bf16_f32 v228, v216, v4\n"
        "v_and_b32 v229, 0xffff, v3\n"
        "s_nop 1\n"
        "v_mfma_f32_32x32x16_bf16 v[192:207], v[228:231], v[224:227], v[192:207]\n"
        "s_nop 7\n"
        ".LA_q:\n"
        "v_mul_f32 v234, 1049335798, v234\n"
        "s_mul_i32 s18, s10, 458752\n"
        "s_add_u32 s18, s18, 131072\n"
        "s_add_u32 s96, s64, s18\n"
        "s_addc_u32 s97, s65, 0\n"
        "s_lshl_b32 s19, s8, 17\n"
        "s_lshl_b32 s18, s10, 15\n"
        "s_add_u32 s18, s18, s19\n"
        "s_add_u32 s18, s18, 2097152\n"
        "s_add_u32 s98, s64, s18\n"
        "s_addc_u32 s99, s65, 0\n"
        "v_pk_mul_f32 v[192:193], v[192:193], v[234:235] op_sel_hi:[1,0]\n"
        "v_pk_mul_f32 v[194:195], v[194:195], v[234:235] op_sel_hi:[1,0]\n"
        "v_pk_mul_f32 v[196:197], v[196:197], v[234:235] op_sel_hi:[1,0]\n"
        "v_pk_mul_f32 v[198:199], v[198:199], v[234:235] op_sel_hi:[1,0]\n"
        "v_pk_mul_f32 v[200:201], v[200:201], v[234:235] op_sel_hi:[1,0]\n"
        "v_pk_mul_f32 v[202:203], v[202:203], v[234:235] op_sel_hi:[1,0]\n"
        "v_pk_mul_f32 v[204:205], v[204:205], v[234:235] op_sel_hi:[1,0]\n"
        "v_pk_mul_f32 v[206:207], v[206:207], v[234:235] op_sel_hi:[1,0]\n"
        "v_cvt_pk_bf16_f32 v96, v192, v193\n"
        "v_cvt_pk_bf16_f32 v97, v194, v195\n"
        "v_cvt_pk_bf16_f32 v98, v196, v197\n"
        "v_cvt_pk_bf16_f32 v99, v198, v199\n"
        "v_cvt_pk_bf16_f32 v100, v200, v201\n"
        "v_cvt_pk_bf16_f32 v101, v202, v203\n"
        "v_cvt_pk_bf16_f32 v102, v204, v205\n"
        "v_cvt_pk_bf16_f32 v103, v206, v207\n"
        "v_cndmask_b32 v102, v102, 0, s[16:17]\n"
        "v_cndmask_b32 v103, v103, 0, s[16:17]\n"
        "ds_write_b128 v252, v[96:99]\n"
        "ds_write_b128 v252, v[100:103] offset:1024\n"
        "s_add_u32 s14, s14, 4\n"
        "s_mov_b64 exec, 1\n"
        "ds_add_u32 v241, v242\n"
        "s_mov_b64 exec, -1\n"
        "s_mov_b32 s26, 0\n"
        ".Lspin_1:\n"
        "ds_read_b32 v1, v241\n"
        "s_waitcnt lgkmcnt(0)\n"
        ".Lsmid_3:\n"
        "v_readfirstlane_b32 s27, v1\n"
        "s_add_u32 s26, s26, 1\n"
        "s_and_b32 s25, s27, 0xffff\n"
        "s_cmp_ge_u32 s25, s14\n"
        "s_cbranch_scc1 .Lsdone_2\n"
        "s_cmp_lt_u32 s26, 400000\n"
        "s_cbranch_scc1 .Lspin_1\n"
        ".Lsdone_2:\n"
        "ds_read_b128 v[120:123], v248\n"
        "ds_read_b128 v[124:127], v248 offset:1024\n"
        "ds_read_b128 v[128:131], v249\n"
        "ds_read_b128 v[132:135], v249 offset:1024\n"
        "ds_read_b128 v[136:139], v250\n"
        "ds_read_b128 v[140:143], v250 offset:1024\n"
        "ds_read_b128 v[144:147], v252\n"
        "ds_read_b128 v[148:151], v252 offset:1024\n"
        "s_cmp_ge_u32 s8, 14\n"
        "s_cbranch_scc1 .LA_nf\n"
        "s_waitcnt vmcnt(0)\n"
        "s_mov_b64 exec, 1\n"
        "ds_add_rtn_u32 v2, v241, v242 offset:4\n"
        "s_mov_b64 exec, -1\n"
        "s_lshl_b32 s18, s8, 4\n"
        "s_add_u32 s18, s18, s9\n"
        "s_lshl_b32 s18, s18, 7\n"
        "s_add_u32 s18, s18, 4096\n"
        "s_waitcnt lgkmcnt(0)\n"
        "v_readfirstlane_b32 s27, v2\n"
        "s_cmp_lg_u32 s27, 3\n"
        "s_cbranch_scc1 .LA_nf\n"
        "v_mov_b32 v2, s68\n"
        "v_mov_b32 v3, s69\n"
        "s_mov_b64 exec, 1\n"
        "buffer_store_dwordx2 v[2:3], off, s[64:67], s18 sc1\n"
        "s_mov_b64 exec, -1\n"
        ".LA_nf:\n"
        "s_mul_i32 s20, s9, 114688\n"
        "s_add_u32 s20, s20, 131072\n"
        "s_add_u32 s18, s64, s20\n"
        "s_addc_u32 s19, s65, 0\n"
        "s_waitcnt lgkmcnt(0)\n"
        "s_sleep 10\n"
        "s_mov_b32 s26, 0\n"
        ".Lpoll_4:\n"
        "buffer_load_dwordx2 v[2:3], v247, s[64:67], 0 offen sc1\n"
        "s_add_u32 s26, s26, 1\n"
        "s_waitcnt vmcnt(0)\n"
        "v_cmp_ne_u64 vcc, s[68:69], v[2:3]\n"
        "s_cbranch_vccz .Lpdone_5\n"
        "s_sleep 2\n"
        "s_cmp_lt_u32 s26, 50000\n"
        "s_cbranch_scc1 .Lpoll_4\n"
        ".Lpdone_5:\n"
        "global_load_dwordx4 v[184:187], v243, s[18:19] sc1\n"
        "global_load_dwordx4 v[188:191], v244, s[18:19] sc1\n"
        "global_load_dwordx4 v[160:163], v243, s[96:97] sc1\n"
        "global_load_dwordx4 v[164:167], v244, s[96:97] sc1\n"
        "global_load_dwordx4 v[192:195], v240, s[98:99] offset:0 sc1\n"
        "global_load_dwordx4 v[196:199], v240, s[98:99] offset:1024 sc1\n"
        "s_add_u32 s96, s96, 28672\n"
        "s_addc_u32 s97, s97, 0\n"
        "s_add_u32 s98, s98, 2048\n"
        "s_addc_u32 s99, s99, 0\n"
        "global_load_dwordx4 v[168:171], v243, s[96:97] sc1\n"
        "global_load_dwordx4 v[172:175], v244, s[96:97] sc1\n"
        "global_load_dwordx4 v[200:203], v240, s[98:99] offset:0 sc1\n"
        "global_load_dwordx4 v[204:207], v240, s[98:99] offset:1024 sc1\n"
        "s_add_u32 s96, s96, 28672\n"
        "s_addc_u32 s97, s97, 0\n"
        "s_add_u32 s98, s98, 2048\n"
        "s_addc_u32 s99, s99, 0\n"
        "global_load_dwordx4 v[176:179], v243, s[96:97] sc1\n"
        "global_load_dwordx4 v[180:183], v244, s[96:97] sc1\n"
        "global_load_dwordx4 v[208:211], v240, s[98:99] offset:0 sc1\n"
        "global_load_dwordx4 v[212:215], v240, s[98:99] offset:1024 sc1\n"
        "s_add_u32 s96, s96, 28672\n"
        "s_addc_u32 s97, s97, 0\n"
        "s_waitcnt vmcnt(12)\n"
        "v_cndmask_b32 v190, v190, v245, s[16:17]\n"
        "v_cndmask_b32 v191, v191, v246, s[16:17]\n"
        "s_nop 1\n"
        "v_mfma_f32_32x32x16_bf16 v[80:95], v[184:187], v[120:123], 0\n"
        "v_mfma_f32_32x32x16_bf16 v[80:95], v[188:191], v[124:127], v[80:95]\n"
        "v_mfma_f32_32x32x16_bf16 v[96:111], v[184:187], v[128:131], 0\n"
        "v_mfma_f32_32x32x16_bf16 v[96:111], v[188:191], v[132:135], v[96:111]\n"
        "s_nop 9\n"
        "v_max3_f32 v232, v80, v81, v82\n"
        "v_max3_f32 v232, v232, v83, v84\n"
        "v_max3_f32 v232, v232, v85, v86\n"
        "v_max3_f32 v232, v232, v87, v88\n"
        "v_max3_f32 v232, v232, v89, v90\n"
        "v_max3_f32 v232, v232, v91, v92\n"
        "v_max3_f32 v232, v232, v93, v94\n"
        "v_max_f32 v232, v232, v95\n"
        "v_mfma_f32_32x32x16_bf16 v[80:95], v[184:187], v[136:139], 0\n"
        "v_mfma_f32_32x32x16_bf16 v[80:95], v[188:191], v[140:143], v[80:95]\n"
        "s_nop 1\n"
        "v_max3_f32 v233, v96, v97, v98\n"
        "v_max3_f32 v233, v233, v99, v100\n"
        "v_max3_f32 v233, v233, v101, v102\n"
        "v_max3_f32 v233, v233, v103, v104\n"
        "v_max3_f32 v233, v233, v105, v106\n"
        "v_max3_f32 v233, v233, v107, v108\n"
        "v_max3_f32 v233, v233, v109, v110\n"
        "v_max_f32 v233, v233, v111\n"
        "v_mfma_f32_32x32x16_bf16 v[96:111], v[184:187], v[144:147], 0\n"
        "v_mfma_f32_32x32x16_bf16 v[96:111], v[188:191], v[148:151], v[96:111]\n"
        "s_nop 1\n"
        "v_max3_f32 v234, v80, v81, v82\n"
        "v_max3_f32 v234, v234, v83, v84\n"
        "v_max3_f32 v234, v234, v85, v86\n"
        "v_max3_f32 v234, v234, v87, v88\n"
        "v_max3_f32 v234, v234, v89, v90\n"
        "v_max3_f32 v234, v234, v91, v92\n"
        "v_max3_f32 v234, v234, v93, v94\n"
        "v_max_f32 v234, v234, v95\n"
        "s_nop 3\n"
        "v_max3_f32 v235, v96, v97, v98\n"
        "v_max3_f32 v235, v235, v99, v100\n"
        "v_max3_f32 v235, v235, v101, v102\n"
        "v_max3_f32 v235, v235, v103, v104\n"
        "v_max3_f32 v235, v235, v105, v106\n"
        "v_max3_f32 v235, v235, v107, v108\n"
        "v_max3_f32 v235, v235, v109, v110\n"
        "v_max_f32 v235, v235, v111\n"
        ".LA_ref:\n"
        "v_mov_b32 v4, v232\n"
        "v_mov_b32 v5, v233\n"
        "v_mov_b32 v6, v234\n"
        "v_mov_b32 v7, v235\n"
        "s_nop 0\n"
        "v_permlane32_swap_b32 v4, v232\n"
        "v_permlane32_swap_b32 v5, v233\n"
        "v_permlane32_swap_b32 v6, v234\n"
        "v_permlane32_swap_b32 v7, v235\n"
        "v_max_f32 v232, v232, v4\n"
        "v_max_f32 v233, v233, v5\n"
        "v_max_f32 v234, v234, v6\n"
        "v_max_f32 v235, v235, v7\n"
        "v_add_f32 v232, s77, v232\n"
        "v_add_f32 v233, s77, v233\n"
        "v_add_f32 v234, s77, v234\n"
        "v_add_f32 v235, s77, v235\n"
        "v_cvt_pk_bf16_f32 v1, -v232, -v232\n"
        "v_and_b32 v1, 0xffff0000, v1\n"
        "v_sub_f32 v1, -v232, v1\n"
        "v_cvt_pk_bf16_f32 v2, v1, v1\n"
        "v_and_b32 v2, 0xffff0000, v2\n"
        "v_sub_f32 v2, v1, v2\n"
        "v_cvt_pk_bf16_f32 v1, -v232, v1\n"
        "v_cvt_pk_bf16_f32 v2, v2, 0\n"
        "v_cndmask_b32 v126, v126, v1, s[16:17]\n"
        "v_cndmask_b32 v127, v127, v2, s[16:17]\n"
        "v_cvt_pk_bf16_f32 v1, -v233, -v233\n"
        "v_and_b32 v1, 0xffff0000, v1\n"
        "v_sub_f32 v1, -v233, v1\n"
        "v_cvt_pk_bf16_f32 v2, v1, v1\n"
        "v_and_b32 v2, 0xffff0000, v2\n"
        "v_sub_f32 v2, v1, v2\n"
        "v_cvt_pk_bf16_f32 v1, -v233, v1\n"
        "v_cvt_pk_bf16_f32 v2, v2, 0\n"
        "v_cndmask_b32 v134, v134, v1, s[16:17]\n"
        "v_cndmask_b32 v135, v135, v2, s[16:17]\n"
        "v_cvt_pk_bf16_f32 v1, -v234, -v234\n"
        "v_and_b32 v1, 0xffff0000, v1\n"
        "v_sub_f32 v1, -v234, v1\n"
        "v_cvt_pk_bf16_f32 v2, v1, v1\n"
        "v_and_b32 v2, 0xffff0000, v2\n"
        "v_sub_f32 v2, v1, v2\n"
        "v_cvt_pk_bf16_f32 v1, -v234, v1\n"
        "v_cvt_pk_bf16_f32 v2, v2, 0\n"
        "v_cndmask_b32 v142, v142, v1, s[16:17]\n"
        "v_cndmask_b32 v143, v143, v2, s[16:17]\n"
        "v_cvt_pk_bf16_f32 v1, -v235, -v235\n"
        "v_and_b32 v1, 0xffff0000, v1\n"
        "v_sub_f32 v1, -v235, v1\n"
        "v_cvt_pk_bf16_f32 v2, v1, v1\n"
        "v_and_b32 v2, 0xffff0000, v2\n"
        "v_sub_f32 v2, v1, v2\n"
        "v_cvt_pk_bf16_f32 v1, -v235, v1\n"
        "v_cvt_pk_bf16_f32 v2, v2, 0\n"
        "v_cndmask_b32 v150, v150, v1, s[16:17]\n"
        "v_cndmask_b32 v151, v151, v2, s[16:17]\n"
        "s_waitcnt vmcnt(8)\n"
        "v_cndmask_b32 v166, v166, v245, s[16:17]\n"
        "v_cndmask_b32 v167, v167, v246, s[16:17]\n"
        "s_mov_b32 s75, 0\n"
        "s_setprio 0\n"
        "v_mfma_f32_32x32x16_bf16 v[80:95], v[160:163], v[120:123], 0\n"
        "v_mfma_f32_32x32x16_bf16 v[80:95], v[164:167], v[124:127], v[80:95]\n"
        "s_nop 10\n"
        ".LA_loop:\n"
        "v_mfma_f32_32x32x16_bf16 v[96:111], v[160:163], v[128:131], 0\n"
        "v_exp_f32 v80, v80\n"
        "v_exp_f32 v81, v81\n"
        "v_exp_f32 v82, v82\n"
        "v_exp_f32 v83, v83\n"
        "v_cvt_pk_bf16_f32 v112, v80, v81\n"
        "v_cvt_pk_bf16_f32 v113, v82, v83\n"
        "v_mfma_f32_32x32x16_bf16 v[96:111], v[164:167], v[132:135], v[96:111]\n"
        "v_exp_f32 v84, v84\n"
        "v_exp_f32 v85, v85\n"
        "v_exp_f32 v86, v86\n"
        "v_exp_f32 v87, v87\n"
        "v_cvt_pk_bf16_f32 v114, v84, v85\n"
        "v_cvt_pk_bf16_f32 v115, v86, v87\n"
        "v_mfma_f32_32x32x16_bf16 v[64:79], v[220:223], v[116:119], v[64:79]\n"
        "v_exp_f32 v88, v88\n"
        "v_exp_f32 v89, v89\n"
        "v_exp_f32 v90, v90\n"
        "v_exp_f32 v91, v91\n"
        "v_cvt_pk_bf16_f32 v116, v88, v89\n"
        "v_cvt_pk_bf16_f32 v117, v90, v91\n"
        "v_mfma_f32_32x32x16_bf16 v[16:31], v[192:195], v[112:115], v[16:31]\n"
        "global_load_dwordx4 v[184:187], v243, s[96:97] sc1\n"
        "v_exp_f32 v92, v92\n"
        "v_exp_f32 v93, v93\n"
        "v_exp_f32 v94, v94\n"
        "v_exp_f32 v95, v95\n"
        "v_cvt_pk_bf16_f32 v118, v92, v93\n"
        "v_cvt_pk_bf16_f32 v119, v94, v95\n"
        "v_mfma_f32_32x32x16_bf16 v[80:95], v[160:163], v[136:139], 0\n"
        "v_exp_f32 v96, v96\n"
        "v_exp_f32 v97, v97\n"
        "v_exp_f32 v98, v98\n"
        "v_exp_f32 v99, v99\n"
        "v_cvt_pk_bf16_f32 v112, v96, v97\n"
        "v_cvt_pk_bf16_f32 v113, v98, v99\n"
        "v_mfma_f32_32x32x16_bf16 v[80:95], v[164:167], v[140:143], v[80:95]\n"
        "s_add_u32 s98, s98, 2048\n"
        "v_exp_f32 v100, v100\n"
        "v_exp_f32 v101, v101\n"
        "v_exp_f32 v102, v102\n"
        "v_exp_f32 v103, v103\n"
        "v_cvt_pk_bf16_f32 v114, v100, v101\n"
        "v_cvt_pk_bf16_f32 v115, v102, v103\n"
        "v_mfma_f32_32x32x16_bf16 v[16:31], v[196:199], v[116:119], v[16:31]\n"
        "s_addc_u32 s99, s99, 0\n"
        "v_exp_f32 v104, v104\n"
        "v_exp_f32 v105, v105\n"
        "v_exp_f32 v106, v106\n"
        "v_exp_f32 v107, v107\n"
        "v_cvt_pk_bf16_f32 v116, v104, v105\n"
        "v_cvt_pk_bf16_f32 v117, v106, v107\n"
        "v_mfma_f32_32x32x16_bf16 v[32:47], v[192:195], v[112:115], v[32:47]\n"
        "global_load_dwordx4 v[188:191], v244, s[96:97] sc1\n"
        "v_exp_f32 v108, v108\n"
        "v_exp_f32 v109, v109\n"
        "v_exp_f32 v110, v110\n"
        "v_exp_f32 v111, v111\n"
        "v_cvt_pk_bf16_f32 v118, v108, v109\n"
        "v_cvt_pk_bf16_f32 v119, v110, v111\n"
        "v_mfma_f32_32x32x16_bf16 v[96:111], v[160:163], v[144:147], 0\n"
        "s_add_u32 s96, s96, 28672\n"
        "v_exp_f32 v80, v80\n"
        "v_exp_f32 v81, v81\n"
        "v_exp_f32 v82, v82\n"
        "v_exp_f32 v83, v83\n"
        "v_cvt_pk_bf16_f32 v112, v80, v81\n"
        "v_cvt_pk_bf16_f32 v113, v82, v83\n"
        "v_mfma_f32_32x32x16_bf16 v[96:111], v[164:167], v[148:151], v[96:111]\n"
        "global_load_dwordx4 v[216:219], v240, s[98:99] offset:0 sc1\n"
        "v_exp_f32 v84, v84\n"
        "v_exp_f32 v85, v85\n"
        "v_exp_f32 v86, v86\n"
        "v_exp_f32 v87, v87\n"
        "v_cvt_pk_bf16_f32 v114, v84, v85\n"
        "v_cvt_pk_bf16_f32 v115, v86, v87\n"
        "v_mfma_f32_32x32x16_bf16 v[32:47], v[196:199], v[116:119], v[32:47]\n"
        "s_waitcnt vmcnt(7)\n"
        "v_exp_f32 v88, v88\n"
        "v_exp_f32 v89, v89\n"
        "v_exp_f32 v90, v90\n"
        "v_exp_f32 v91, v91\n"
        "v_cvt_pk_bf16_f32 v116, v88, v89\n"
        "v_cvt_pk_bf16_f32 v117, v90, v91\n"
        "v_cndmask_b32 v174, v174, v245, s[16:17]\n"
        "v_cndmask_b32 v175, v175, v246, s[16:17]\n"
        "v_mfma_f32_32x32x16_bf16 v[48:63], v[192:195], v[112:115], v[48:63]\n"
        "s_addc_u32 s97, s97, 0\n"
        "v_exp_f32 v92, v92\n"
        "v_exp_f32 v93, v93\n"
        "v_exp_f32 v94, v94\n"
        "v_exp_f32 v95, v95\n"
        "v_cvt_pk_bf16_f32 v118, v92, v93\n"
        "v_cvt_pk_bf16_f32 v119, v94, v95\n"
        "v_mfma_f32_32x32x16_bf16 v[80:95], v[168:171], v[120:123], 0\n"
        "v_exp_f32 v96, v96\n"
        "v_exp_f32 v97, v97\n"
        "v_exp_f32 v98, v98\n"
        "v_exp_f32 v99, v99\n"
        "v_cvt_pk_bf16_f32 v112, v96, v97\n"
        "v_cvt_pk_bf16_f32 v113, v98, v99\n"
        "v_mfma_f32_32x32x16_bf16 v[80:95], v[172:175], v[124:127], v[80:95]\n"
        "global_load_dwordx4 v[220:223], v240, s[98:99] offset:1024 sc1\n"
        "v_exp_f32 v100, v100\n"
        "v_exp_f32 v101, v101\n"
        "v_exp_f32 v102, v102\n"
        "v_exp_f32 v103, v103\n"
        "v_cvt_pk_bf16_f32 v114, v100, v101\n"
        "v_cvt_pk_bf16_f32 v115, v102, v103\n"
        "v_mfma_f32_32x32x16_bf16 v[48:63], v[196:199], v[116:119], v[48:63]\n"
        "v_exp_f32 v104, v104\n"
        "v_exp_f32 v105, v105\n"
        "v_exp_f32 v106, v106\n"
        "v_exp_f32 v107, v107\n"
        "v_cvt_pk_bf16_f32 v116, v104, v105\n"
        "v_cvt_pk_bf16_f32 v117, v106, v107\n"
        "v_mfma_f32_32x32x16_bf16 v[64:79], v[192:195], v[112:115], v[64:79]\n"
        "v_exp_f32 v108, v108\n"
        "v_exp_f32 v109, v109\n"
        "v_exp_f32 v110, v110\n"
        "v_exp_f32 v111, v111\n"
        "v_cvt_pk_bf16_f32 v118, v108, v109\n"
        "v_cvt_pk_bf16_f32 v119, v110, v111\n"
        "v_mfma_f32_32x32x16_bf16 v[96:111], v[168:171], v[128:131], 0\n"
        "v_exp_f32 v80, v80\n"
        "v_exp_f32 v81, v81\n"
        "v_exp_f32 v82, v82\n"
        "v_exp_f32 v83, v83\n"
        "v_cvt_pk_bf16_f32 v112, v80, v81\n"
        "v_cvt_pk_bf16_f32 v113, v82, v83\n"
        "v_mfma_f32_32x32x16_bf16 v[96:111], v[172:175], v[132:135], v[96:111]\n"
        "v_exp_f32 v84, v84\n"
        "v_exp_f32 v85, v85\n"
        "v_exp_f32 v86, v86\n"
        "v_exp_f32 v87, v87\n"
        "v_cvt_pk_bf16_f32 v114, v84, v85\n"
        "v_cvt_pk_bf16_f32 v115, v86, v87\n"
        "v_mfma_f32_32x32x16_bf16 v[64:79], v[196:199], v[116:119], v[64:79]\n"
        "v_exp_f32 v88, v88\n"
        "v_exp_f32 v89, v89\n"
        "v_exp_f32 v90, v90\n"
        "v_exp_f32 v91, v91\n"
        "v_cvt_pk_bf16_f32 v116, v88, v89\n"
        "v_cvt_pk_bf16_f32 v117, v90, v91\n"
        "v_mfma_f32_32x32x16_bf16 v[16:31], v[200:203], v[112:115], v[16:31]\n"
        "global_load_dwordx4 v[160:163], v243, s[96:97] sc1\n"
        "v_exp_f32 v92, v92\n"
        "v_exp_f32 v93, v93\n"
        "v_exp_f32 v94, v94\n"
        "v_exp_f32 v95, v95\n"
        "v_cvt_pk_bf16_f32 v118, v92, v93\n"
        "v_cvt_pk_bf16_f32 v119, v94, v95\n"
        "v_mfma_f32_32x32x16_bf16 v[80:95], v[168:171], v[136:139], 0\n"
        "v_exp_f32 v96, v96\n"
        "v_exp_f32 v97, v97\n"
        "v_exp_f32 v98, v98\n"
        "v_exp_f32 v99, v99\n"
        "v_cvt_pk_bf16_f32 v112, v96, v97\n"
        "v_cvt_pk_bf16_f32 v113, v98, v99\n"
        "v_mfma_f32_32x32x16_bf16 v[80:95], v[172:175], v[140:143], v[80:95]\n"
        "s_add_u32 s98, s98, 2048\n"
        "v_exp_f32 v100, v100\n"
        "v_exp_f32 v101, v101\n"
        "v_exp_f32 v102, v102\n"
        "v_exp_f32 v103, v103\n"
        "v_cvt_pk_bf16_f32 v114, v100, v101\n"
        "v_cvt_pk_bf16_f32 v115, v102, v103\n"
        "v_mfma_f32_32x32x16_bf16 v[16:31], v[204:207], v[116:119], v[16:31]\n"
        "s_addc_u32 s99, s99, 0\n"
        "v_exp_f32 v104, v104\n"
        "v_exp_f32 v105, v105\n"
        "v_exp_f32 v106, v106\n"
        "v_exp_f32 v107, v107\n"
        "v_cvt_pk_bf16_f32 v116, v104, v105\n"
        "v_cvt_pk_bf16_f32 v117, v106, v107\n"
        "v_mfma_f32_32x32x16_bf16 v[32:47], v[200:203], v[112:115], v[32:47]\n"
        "global_load_dwordx4 v[164:167], v244, s[96:97] sc1\n"
        "v_exp_f32 v108, v108\n"
        "v_exp_f32 v109, v109\n"
        "v_exp_f32 v110, v110\n"
        "v_exp_f32 v111, v111\n"
        "v_cvt_pk_bf16_f32 v118, v108, v109\n"
        "v_cvt_pk_bf16_f32 v119, v110, v111\n"
        "v_mfma_f32_32x32x16_bf16 v[96:111], v[168:171], v[144:147], 0\n"
        "s_add_u32 s96, s96, 28672\n"
        "v_exp_f32 v80, v80\n"
        "v_exp_f32 v81, v81\n"
        "v_exp_f32 v82, v82\n"
        "v_exp_f32 v83, v83\n"
        "v_cvt_pk_bf16_f32 v112, v80, v81\n"
        "v_cvt_pk_bf16_f32 v113, v82, v83\n"
        "v_mfma_f32_32x32x16_bf16 v[96:111], v[172:175], v[148:151], v[96:111]\n"
        "global_load_dwordx4 v[192:195], v240, s[98:99] offset:0 sc1\n"
        "v_exp_f32 v84, v84\n"
        "v_exp_f32 v85, v85\n"
        "v_exp_f32 v86, v86\n"
        "v_exp_f32 v87, v87\n"
        "v_cvt_pk_bf16_f32 v114, v84, v85\n"
        "v_cvt_pk_bf16_f32 v115, v86, v87\n"
        "v_mfma_f32_32x32x16_bf16 v[32:47], v[204:207], v[116:119], v[32:47]\n"
        "s_waitcnt vmcnt(7)\n"
        "v_exp_f32 v88, v88\n"
        "v_exp_f32 v89, v89\n"
        "v_exp_f32 v90, v90\n"
        "v_exp_f32 v91, v91\n"
        "v_cvt_pk_bf16_f32 v116, v88, v89\n"
        "v_cvt_pk_bf16_f32 v117, v90, v91\n"
        "v_cndmask_b32 v182, v182, v245, s[16:17]\n"
        "v_cndmask_b32 v183, v183, v246, s[16:17]\n"
        "v_mfma_f32_32x32x16_bf16 v[48:63], v[200:203], v[112:115], v[48:63]\n"
        "s_addc_u32 s97, s97, 0\n"
        "v_exp_f32 v92, v92\n"
        "v_exp_f32 v93, v93\n"
        "v_exp_f32 v94, v94\n"
        "v_exp_f32 v95, v95\n"
        "v_cvt_pk_bf16_f32 v118, v92, v93\n"
        "v_cvt_pk_bf16_f32 v119, v94, v95\n"
        "v_mfma_f32_32x32x16_bf16 v[80:95], v[176:179], v[120:123], 0\n"
        "v_exp_f32 v96, v96\n"
        "v_exp_f32 v97, v97\n"
        "v_exp_f32 v98, v98\n"
        "v_exp_f32 v99, v99\n"
        "v_cvt_pk_bf16_f32 v112, v96, v97\n"
        "v_cvt_pk_bf16_f32 v113, v98, v99\n"
        "v_mfma_f32_32x32x16_bf16 v[80:95], v[180:183], v[124:127], v[80:95]\n"
        "global_load_dwordx4 v[196:199], v240, s[98:99] offset:1024 sc1\n"
        "v_exp_f32 v100, v100\n"
        "v_exp_f32 v101, v101\n"
        "v_exp_f32 v102, v102\n"
        "v_exp_f32 v103, v103\n"
        "v_cvt_pk_bf16_f32 v114, v100, v101\n"
        "v_cvt_pk_bf16_f32 v115, v102, v103\n"
        "v_mfma_f32_32x32x16_bf16 v[48:63], v[204:207], v[116:119], v[48:63]\n"
        "v_exp_f32 v104, v104\n"
        "v_exp_f32 v105, v105\n"
        "v_exp_f32 v106, v106\n"
        "v_exp_f32 v107, v107\n"
        "v_cvt_pk_bf16_f32 v116, v104, v105\n"
        "v_cvt_pk_bf16_f32 v117, v106, v107\n"
        "v_mfma_f32_32x32x16_bf16 v[64:79], v[200:203], v[112:115], v[64:79]\n"
        "v_exp_f32 v108, v108\n"
        "v_exp_f32 v109, v109\n"
        "v_exp_f32 v110, v110\n"
        "v_exp_f32 v111, v111\n"
        "v_cvt_pk_bf16_f32 v118, v108, v109\n"
        "v_cvt_pk_bf16_f32 v119, v110, v111\n"
        "v_mfma_f32_32x32x16_bf16 v[96:111], v[176:179], v[128:131], 0\n"
        "v_exp_f32 v80, v80\n"
        "v_exp_f32 v81, v81\n"
        "v_exp_f32 v82, v82\n"
        "v_exp_f32 v83, v83\n"
        "v_cvt_pk_bf16_f32 v112, v80, v81\n"
        "v_cvt_pk_bf16_f32 v113, v82, v83\n"
        "v_mfma_f32_32x32x16_bf16 v[96:111], v[180:183], v[132:135], v[96:111]\n"
        "v_exp_f32 v84, v84\n"
        "v_exp_f32 v85, v85\n"
        "v_exp_f32 v86, v86\n"
        "v_exp_f32 v87, v87\n"
        "v_cvt_pk_bf16_f32 v114, v84, v85\n"
        "v_cvt_pk_bf16_f32 v115, v86, v87\n"
        "v_mfma_f32_32x32x16_bf16 v[64:79], v[204:207], v[116:119], v[64:79]\n"
        "v_exp_f32 v88, v88\n"
        "v_exp_f32 v89, v89\n"
        "v_exp_f32 v90, v90\n"
        "v_exp_f32 v91, v91\n"
        "v_cvt_pk_bf16_f32 v116, v88, v89\n"
        "v_cvt_pk_bf16_f32 v117, v90, v91\n"
        "v_mfma_f32_32x32x16_bf16 v[16:31], v[208:211], v[112:115], v[16:31]\n"
        "global_load_dwordx4 v[168:171], v243, s[96:97] sc1\n"
        "v_exp_f32 v92, v92\n"
        "v_exp_f32 v93, v93\n"
        "v_exp_f32 v94, v94\n"
        "v_exp_f32 v95, v95\n"
        "v_cvt_pk_bf16_f32 v118, v92, v93\n"
        "v_cvt_pk_bf16_f32 v119, v94, v95\n"
        "v_mfma_f32_32x32x16_bf16 v[80:95], v[176:179], v[136:139], 0\n"
        "v_exp_f32 v96, v96\n"
        "v_exp_f32 v97, v97\n"
        "v_exp_f32 v98, v98\n"
        "v_exp_f32 v99, v99\n"
        "v_cvt_pk_bf16_f32 v112, v96, v97\n"
        "v_cvt_pk_bf16_f32 v113, v98, v99\n"
        "v_mfma_f32_32x32x16_bf16 v[80:95], v[180:183], v[140:143], v[80:95]\n"
        "s_add_u32 s98, s98, 2048\n"
        "v_exp_f32 v100, v100\n"
        "v_exp_f32 v101, v101\n"
        "v_exp_f32 v102, v102\n"
        "v_exp_f32 v103, v103\n"
        "v_cvt_pk_bf16_f32 v114, v100, v101\n"
        "v_cvt_pk_bf16_f32 v115, v102, v103\n"
        "v_mfma_f32_32x32x16_bf16 v[16:31], v[212:215], v[116:119], v[16:31]\n"
        "s_addc_u32 s99, s99, 0\n"
        "v_exp_f32 v104, v104\n"
        "v_exp_f32 v105, v105\n"
        "v_exp_f32 v106, v106\n"
        "v_exp_f32 v107, v107\n"
        "v_cvt_pk_bf16_f32 v116, v104, v105\n"
        "v_cvt_pk_bf16_f32 v117, v106, v107\n"
        "v_mfma_f32_32x32x16_bf16 v[32:47], v[208:211], v[112:115], v[32:47]\n"
        "global_load_dwordx4 v[172:175], v244, s[96:97] sc1\n"
        "v_exp_f32 v108, v108\n"
        "v_exp_f32 v109, v109\n"
        "v_exp_f32 v110, v110\n"
        "v_exp_f32 v111, v111\n"
        "v_cvt_pk_bf16_f32 v118, v108, v109\n"
        "v_cvt_pk_bf16_f32 v119, v110, v111\n"
        "v_mfma_f32_32x32x16_bf16 v[96:111], v[176:179], v[144:147], 0\n"
        "s_add_u32 s96, s96, 28672\n"
        "v_exp_f32 v80, v80\n"
        "v_exp_f32 v81, v81\n"
        "v_exp_f32 v82, v82\n"
        "v_exp_f32 v83, v83\n"
        "v_cvt_pk_bf16_f32 v112, v80, v81\n"
        "v_cvt_pk_bf16_f32 v113, v82, v83\n"
        "v_mfma_f32_32x32x16_bf16 v[96:111], v[180:183], v[148:151], v[96:111]\n"
        "global_load_dwordx4 v[200:203], v240, s[98:99] offset:0 sc1\n"
        "v_exp_f32 v84, v84\n"
        "v_exp_f32 v85, v85\n"
        "v_exp_f32 v86, v86\n"
        "v_exp_f32 v87, v87\n"
        "v_cvt_pk_bf16_f32 v114, v84, v85\n"
        "v_cvt_pk_bf16_f32 v115, v86, v87\n"
        "v_mfma_f32_32x32x16_bf16 v[32:47], v[212:215], v[116:119], v[32:47]\n"
        "s_waitcnt vmcnt(7)\n"
        "v_exp_f32 v88, v88\n"
        "v_exp_f32 v89, v89\n"
        "v_exp_f32 v90, v90\n"
        "v_exp_f32 v91, v91\n"
        "v_cvt_pk_bf16_f32 v116, v88, v89\n"
        "v_cvt_pk_bf16_f32 v117, v90, v91\n"
        "v_cndmask_b32 v190, v190, v245, s[16:17]\n"
        "v_cndmask_b32 v191, v191, v246, s[16:17]\n"
        "v_mfma_f32_32x32x16_bf16 v[48:63], v[208:211], v[112:115], v[48:63]\n"
        "s_addc_u32 s97, s97, 0\n"
        "v_exp_f32 v92, v92\n"
        "v_exp_f32 v93, v93\n"
        "v_exp_f32 v94, v94\n"
        "v_exp_f32 v95, v95\n"
        "v_cvt_pk_bf16_f32 v118, v92, v93\n"
        "v_cvt_pk_bf16_f32 v119, v94, v95\n"
        "v_mfma_f32_32x32x16_bf16 v[80:95], v[184:187], v[120:123], 0\n"
        "v_exp_f32 v96, v96\n"
        "v_exp_f32 v97, v97\n"
        "v_exp_f32 v98, v98\n"
        "v_exp_f32 v99, v99\n"
        "v_cvt_pk_bf16_f32 v112, v96, v97\n"
        "v_cvt_pk_bf16_f32 v113, v98, v99\n"
        "v_mfma_f32_32x32x16_bf16 v[80:95], v[188:191], v[124:127], v[80:95]\n"
        "global_load_dwordx4 v[204:207], v240, s[98:99] offset:1024 sc1\n"
        "v_exp_f32 v100, v100\n"
        "v_exp_f32 v101, v101\n"
        "v_exp_f32 v102, v102\n"
        "v_exp_f32 v103, v103\n"
        "v_cvt_pk_bf16_f32 v114, v100, v101\n"
        "v_cvt_pk_bf16_f32 v115, v102, v103\n"
        "v_mfma_f32_32x32x16_bf16 v[48:63], v[212:215], v[116:119], v[48:63]\n"
        "v_exp_f32 v104, v104\n"
        "v_exp_f32 v105, v105\n"
        "v_exp_f32 v106, v106\n"
        "v_exp_f32 v107, v107\n"
        "v_cvt_pk_bf16_f32 v116, v104, v105\n"
        "v_cvt_pk_bf16_f32 v117, v106, v107\n"
        "v_mfma_f32_32x32x16_bf16 v[64:79], v[208:211], v[112:115], v[64:79]\n"
        "v_exp_f32 v108, v108\n"
        "v_exp_f32 v109, v109\n"
        "v_exp_f32 v110, v110\n"
        "v_exp_f32 v111, v111\n"
        "v_cvt_pk_bf16_f32 v118, v108, v109\n"
        "v_cvt_pk_bf16_f32 v119, v110, v111\n"
        "v_mfma_f32_32x32x16_bf16 v[96:111], v[184:187], v[128:131], 0\n"
        "v_exp_f32 v80, v80\n"
        "v_exp_f32 v81, v81\n"
        "v_exp_f32 v82, v82\n"
        "v_exp_f32 v83, v83\n"
        "v_cvt_pk_bf16_f32 v112, v80, v81\n"
        "v_cvt_pk_bf16_f32 v113, v82, v83\n"
        "v_mfma_f32_32x32x16_bf16 v[96:111], v[188:191], v[132:135], v[96:111]\n"
        "v_exp_f32 v84, v84\n"
        "v_exp_f32 v85, v85\n"
        "v_exp_f32 v86, v86\n"
        "v_exp_f32 v87, v87\n"
        "v_cvt_pk_bf16_f32 v114, v84, v85\n"
        "v_cvt_pk_bf16_f32 v115, v86, v87\n"
        "v_mfma_f32_32x32x16_bf16 v[64:79], v[212:215], v[116:119], v[64:79]\n"
        "v_exp_f32 v88, v88\n"
        "v_exp_f32 v89, v89\n"
        "v_exp_f32 v90, v90\n"
        "v_exp_f32 v91, v91\n"
        "v_cvt_pk_bf16_f32 v116, v88, v89\n"
        "v_cvt_pk_bf16_f32 v117, v90, v91\n"
        "v_mfma_f32_32x32x16_bf16 v[16:31], v[216:219], v[112:115], v[16:31]\n"
        "global_load_dwordx4 v[176:179], v243, s[96:97] sc1\n"
        "v_exp_f32 v92, v92\n"
        "v_exp_f32 v93, v93\n"
        "v_exp_f32 v94, v94\n"
        "v_exp_f32 v95, v95\n"
        "v_cvt_pk_bf16_f32 v118, v92, v93\n"
        "v_cvt_pk_bf16_f32 v119, v94, v95\n"
        "v_mfma_f32_32x32x16_bf16 v[80:95], v[184:187], v[136:139], 0\n"
        "s_add_u32 s75, s75, 1\n"
        "v_exp_f32 v96, v96\n"
        "v_exp_f32 v97, v97\n"
        "v_exp_f32 v98, v98\n"
        "v_exp_f32 v99, v99\n"
        "v_cvt_pk_bf16_f32 v112, v96, v97\n"
        "v_cvt_pk_bf16_f32 v113, v98, v99\n"
        "v_mfma_f32_32x32x16_bf16 v[80:95], v[188:191], v[140:143], v[80:95]\n"
        "s_add_u32 s98, s98, 2048\n"
        "v_exp_f32 v100, v100\n"
        "v_exp_f32 v101, v101\n"
        "v_exp_f32 v102, v102\n"
        "v_exp_f32 v103, v103\n"
        "v_cvt_pk_bf16_f32 v114, v100, v101\n"
        "v_cvt_pk_bf16_f32 v115, v102, v103\n"
        "v_mfma_f32_32x32x16_bf16 v[16:31], v[220:223], v[116:119], v[16:31]\n"
        "s_addc_u32 s99, s99, 0\n"
        "v_exp_f32 v104, v104\n"
        "v_exp_f32 v105, v105\n"
        "v_exp_f32 v106, v106\n"
        "v_exp_f32 v107, v107\n"
        "v_cvt_pk_bf16_f32 v116, v104, v105\n"
        "v_cvt_pk_bf16_f32 v117, v106, v107\n"
        "v_mfma_f32_32x32x16_bf16 v[32:47], v[216:219], v[112:115], v[32:47]\n"
        "global_load_dwordx4 v[180:183], v244, s[96:97] sc1\n"
        "v_exp_f32 v108, v108\n"
        "v_exp_f32 v109, v109\n"
        "v_exp_f32 v110, v110\n"
        "v_exp_f32 v111, v111\n"
        "v_cvt_pk_bf16_f32 v118, v108, v109\n"
        "v_cvt_pk_bf16_f32 v119, v110, v111\n"
        "v_mfma_f32_32x32x16_bf16 v[96:111], v[184:187], v[144:147], 0\n"
        "s_add_u32 s96, s96, 28672\n"
        "v_exp_f32 v80, v80\n"
        "v_exp_f32 v81, v81\n"
        "v_exp_f32 v82, v82\n"
        "v_exp_f32 v83, v83\n"
        "v_cvt_pk_bf16_f32 v112, v80, v81\n"
        "v_cvt_pk_bf16_f32 v113, v82, v83\n"
        "v_mfma_f32_32x32x16_bf16 v[96:111], v[188:191], v[148:151], v[96:111]\n"
        "global_load_dwordx4 v[208:211], v240, s[98:99] offset:0 sc1\n"
        "v_exp_f32 v84, v84\n"
        "v_exp_f32 v85, v85\n"
        "v_exp_f32 v86, v86\n"
        "v_exp_f32 v87, v87\n"
        "v_cvt_pk_bf16_f32 v114, v84, v85\n"
        "v_cvt_pk_bf16_f32 v115, v86, v87\n"
        "v_mfma_f32_32x32x16_bf16 v[32:47], v[220:223], v[116:119], v[32:47]\n"
        "s_waitcnt vmcnt(7)\n"
        "v_exp_f32 v88, v88\n"
        "v_exp_f32 v89, v89\n"
        "v_exp_f32 v90, v90\n"
        "v_exp_f32 v91, v91\n"
        "v_cvt_pk_bf16_f32 v116, v88, v89\n"
        "v_cvt_pk_bf16_f32 v117, v90, v91\n"
        "v_cndmask_b32 v166, v166, v245, s[16:17]\n"
        "v_cndmask_b32 v167, v167, v246, s[16:17]\n"
        "v_mfma_f32_32x32x16_bf16 v[48:63], v[216:219], v[112:115], v[48:63]\n"
        "s_addc_u32 s97, s97, 0\n"
        "v_exp_f32 v92, v92\n"
        "v_exp_f32 v93, v93\n"
        "v_exp_f32 v94, v94\n"
        "v_exp_f32 v95, v95\n"
        "v_cvt_pk_bf16_f32 v118, v92, v93\n"
        "v_cvt_pk_bf16_f32 v119, v94, v95\n"
        "v_mfma_f32_32x32x16_bf16 v[80:95], v[160:163], v[120:123], 0\n"
        "v_exp_f32 v96, v96\n"
        "v_exp_f32 v97, v97\n"
        "v_exp_f32 v98, v98\n"
        "v_exp_f32 v99, v99\n"
        "v_cvt_pk_bf16_f32 v112, v96, v97\n"
        "v_cvt_pk_bf16_f32 v113, v98, v99\n"
        "v_mfma_f32_32x32x16_bf16 v[80:95], v[164:167], v[124:127], v[80:95]\n"
        "global_load_dwordx4 v[212:215], v240, s[98:99] offset:1024 sc1\n"
        "v_exp_f32 v100, v100\n"
        "v_exp_f32 v101, v101\n"
        "v_exp_f32 v102, v102\n"
        "v_exp_f32 v103, v103\n"
        "v_cvt_pk_bf16_f32 v114, v100, v101\n"
        "v_cvt_pk_bf16_f32 v115, v102, v103\n"
        "v_mfma_f32_32x32x16_bf16 v[48:63], v[220:223], v[116:119], v[48:63]\n"
        "v_exp_f32 v104, v104\n"
        "v_exp_f32 v105, v105\n"
        "v_exp_f32 v106, v106\n"
        "v_exp_f32 v107, v107\n"
        "v_cvt_pk_bf16_f32 v116, v104, v105\n"
        "v_cvt_pk_bf16_f32 v117, v106, v107\n"
        "v_mfma_f32_32x32x16_bf16 v[64:79], v[216:219], v[112:115], v[64:79]\n"
        "s_cmp_lt_u32 s75, 3\n"
        "v_exp_f32 v108, v108\n"
        "v_exp_f32 v109, v109\n"
        "v_exp_f32 v110, v110\n"
        "v_exp_f32 v111, v111\n"
        "v_cvt_pk_bf16_f32 v118, v108, v109\n"
        "v_cvt_pk_bf16_f32 v119, v110, v111\n"
        "s_cbranch_scc1 .LA_loop\n"
        "v_mfma_f32_32x32x16_bf16 v[96:111], v[160:163], v[128:131], 0\n"
        "v_exp_f32 v80, v80\n"
        "v_exp_f32 v81, v81\n"
        "v_exp_f32 v82, v82\n"
        "v_exp_f32 v83, v83\n"
        "v_cvt_pk_bf16_f32 v112, v80, v81\n"
        "v_cvt_pk_bf16_f32 v113, v82, v83\n"
        "v_mfma_f32_32x32x16_bf16 v[96:111], v[164:167], v[132:135], v[96:111]\n"
        "v_exp_f32 v84, v84\n"
        "v_exp_f32 v85, v85\n"
        "v_exp_f32 v86, v86\n"
        "v_exp_f32 v87, v87\n"
        "v_cvt_pk_bf16_f32 v114, v84, v85\n"
        "v_cvt_pk_bf16_f32 v115, v86, v87\n"
        "v_mfma_f32_32x32x16_bf16 v[64:79], v[220:223], v[116:119], v[64:79]\n"
        "v_exp_f32 v88, v88\n"
        "v_exp_f32 v89, v89\n"
        "v_exp_f32 v90, v90\n"
        "v_exp_f32 v91, v91\n"
        "v_cvt_pk_bf16_f32 v116, v88, v89\n"
        "v_cvt_pk_bf16_f32 v117, v90, v91\n"
        "v_mfma_f32_32x32x16_bf16 v[16:31], v[192:195], v[112:115], v[16:31]\n"
        "global_load_dwordx4 v[184:187], v243, s[96:97] sc1\n"
        "v_exp_f32 v92, v92\n"
        "v_exp_f32 v93, v93\n"
        "v_exp_f32 v94, v94\n"
        "v_exp_f32 v95, v95\n"
        "v_cvt_pk_bf16_f32 v118, v92, v93\n"
        "v_cvt_pk_bf16_f32 v119, v94, v95\n"
        "v_mfma_f32_32x32x16_bf16 v[80:95], v[160:163], v[136:139], 0\n"
        "v_exp_f32 v96, v96\n"
        "v_exp_f32 v97, v97\n"
        "v_exp_f32 v98, v98\n"
        "v_exp_f32 v99, v99\n"
        "v_cvt_pk_bf16_f32 v112, v96, v97\n"
        "v_cvt_pk_bf16_f32 v113, v98, v99\n"
        "v_mfma_f32_32x32x16_bf16 v[80:95], v[164:167], v[140:143], v[80:95]\n"
        "s_add_u32 s98, s98, 2048\n"
        "v_exp_f32 v100, v100\n"
        "v_exp_f32 v101, v101\n"
        "v_exp_f32 v102, v102\n"
        "v_exp_f32 v103, v103\n"
        "v_cvt_pk_bf16_f32 v114, v100, v101\n"
        "v_cvt_pk_bf16_f32 v115, v102, v103\n"
        "v_mfma_f32_32x32x16_bf16 v[16:31], v[196:199], v[116:119], v[16:31]\n"
        "s_addc_u32 s99, s99, 0\n"
        "v_exp_f32 v104, v104\n"
        "v_exp_f32 v105, v105\n"
        "v_exp_f32 v106, v106\n"
        "v_exp_f32 v107, v107\n"
        "v_cvt_pk_bf16_f32 v116, v104, v105\n"
        "v_cvt_pk_bf16_f32 v117, v106, v107\n"
        "v_mfma_f32_32x32x16_bf16 v[32:47], v[192:195], v[112:115], v[32:47]\n"
        "global_load_dwordx4 v[188:191], v244, s[96:97] sc1\n"
        "v_exp_f32 v108, v108\n"
        "v_exp_f32 v109, v109\n"
        "v_exp_f32 v110, v110\n"
        "v_exp_f32 v111, v111\n"
        "v_cvt_pk_bf16_f32 v118, v108, v109\n"
        "v_cvt_pk_bf16_f32 v119, v110, v111\n"
        "v_mfma_f32_32x32x16_bf16 v[96:111], v[160:163], v[144:147], 0\n"
        "s_add_u32 s96, s96, 28672\n"
        "v_exp_f32 v80, v80\n"
        "v_exp_f32 v81, v81\n"
        "v_exp_f32 v82, v82\n"
        "v_exp_f32 v83, v83\n"
        "v_cvt_pk_bf16_f32 v112, v80, v81\n"
        "v_cvt_pk_bf16_f32 v113, v82, v83\n"
        "v_mfma_f32_32x32x16_bf16 v[96:111], v[164:167], v[148:151], v[96:111]\n"
        "global_load_dwordx4 v[216:219], v240, s[98:99] offset:0 sc1\n"
        "v_exp_f32 v84, v84\n"
        "v_exp_f32 v85, v85\n"
        "v_exp_f32 v86, v86\n"
        "v_exp_f32 v87, v87\n"
        "v_cvt_pk_bf16_f32 v114, v84, v85\n"
        "v_cvt_pk_bf16_f32 v115, v86, v87\n"
        "v_mfma_f32_32x32x16_bf16 v[32:47], v[196:199], v[116:119], v[32:47]\n"
        "s_waitcnt vmcnt(7)\n"
        "v_exp_f32 v88, v88\n"
        "v_exp_f32 v89, v89\n"
        "v_exp_f32 v90, v90\n"
        "v_exp_f32 v91, v91\n"
        "v_cvt_pk_bf16_f32 v116, v88, v89\n"
        "v_cvt_pk_bf16_f32 v117, v90, v91\n"
        "v_cndmask_b32 v174, v174, v245, s[16:17]\n"
        "v_cndmask_b32 v175, v175, v246, s[16:17]\n"
        "v_mfma_f32_32x32x16_bf16 v[48:63], v[192:195], v[112:115], v[48:63]\n"
        "s_addc_u32 s97, s97, 0\n"
        "v_exp_f32 v92, v92\n"
        "v_exp_f32 v93, v93\n"
        "v_exp_f32 v94, v94\n"
        "v_exp_f32 v95, v95\n"
        "v_cvt_pk_bf16_f32 v118, v92, v93\n"
        "v_cvt_pk_bf16_f32 v119, v94, v95\n"
        "v_mfma_f32_32x32x16_bf16 v[80:95], v[168:171], v[120:123], 0\n"
        "v_exp_f32 v96, v96\n"
        "v_exp_f32 v97, v97\n"
        "v_exp_f32 v98, v98\n"
        "v_exp_f32 v99, v99\n"
        "v_cvt_pk_bf16_f32 v112, v96, v97\n"
        "v_cvt_pk_bf16_f32 v113, v98, v99\n"
        "v_mfma_f32_32x32x16_bf16 v[80:95], v[172:175], v[124:127], v[80:95]\n"
        "global_load_dwordx4 v[220:223], v240, s[98:99] offset:1024 sc1\n"
        "v_exp_f32 v100, v100\n"
        "v_exp_f32 v101, v101\n"
        "v_exp_f32 v102, v102\n"
        "v_exp_f32 v103, v103\n"
        "v_cvt_pk_bf16_f32 v114, v100, v101\n"
        "v_cvt_pk_bf16_f32 v115, v102, v103\n"
        "v_mfma_f32_32x32x16_bf16 v[48:63], v[196:199], v[116:119], v[48:63]\n"
        "v_exp_f32 v104, v104\n"
        "v_exp_f32 v105, v105\n"
        "v_exp_f32 v106, v106\n"
        "v_exp_f32 v107, v107\n"
        "v_cvt_pk_bf16_f32 v116, v104, v105\n"
        "v_cvt_pk_bf16_f32 v117, v106, v107\n"
        "v_mfma_f32_32x32x16_bf16 v[64:79], v[192:195], v[112:115], v[64:79]\n"
        "v_exp_f32 v108, v108\n"
        "v_exp_f32 v109, v109\n"
        "v_exp_f32 v110, v110\n"
        "v_exp_f32 v111, v111\n"
        "v_cvt_pk_bf16_f32 v118, v108, v109\n"
        "v_cvt_pk_bf16_f32 v119, v110, v111\n"
        "v_mfma_f32_32x32x16_bf16 v[96:111], v[168:171], v[128:131], 0\n"
        "v_exp_f32 v80, v80\n"
        "v_exp_f32 v81, v81\n"
        "v_exp_f32 v82, v82\n"
        "v_exp_f32 v83, v83\n"
        "v_cvt_pk_bf16_f32 v112, v80, v81\n"
        "v_cvt_pk_bf16_f32 v113, v82, v83\n"
        "v_mfma_f32_32x32x16_bf16 v[96:111], v[172:175], v[132:135], v[96:111]\n"
        "v_exp_f32 v84, v84\n"
        "v_exp_f32 v85, v85\n"
        "v_exp_f32 v86, v86\n"
        "v_exp_f32 v87, v87\n"
        "v_cvt_pk_bf16_f32 v114, v84, v85\n"
        "v_cvt_pk_bf16_f32 v115, v86, v87\n"
        "v_mfma_f32_32x32x16_bf16 v[64:79], v[196:199], v[116:119], v[64:79]\n"
        "v_exp_f32 v88, v88\n"
        "v_exp_f32 v89, v89\n"
        "v_exp_f32 v90, v90\n"
        "v_exp_f32 v91, v91\n"
        "v_cvt_pk_bf16_f32 v116, v88, v89\n"
        "v_cvt_pk_bf16_f32 v117, v90, v91\n"
        "v_mfma_f32_32x32x16_bf16 v[16:31], v[200:203], v[112:115], v[16:31]\n"
        "v_exp_f32 v92, v92\n"
        "v_exp_f32 v93, v93\n"
        "v_exp_f32 v94, v94\n"
        "v_exp_f32 v95, v95\n"
        "v_cvt_pk_bf16_f32 v118, v92, v93\n"
        "v_cvt_pk_bf16_f32 v119, v94, v95\n"
        "v_mfma_f32_32x32x16_bf16 v[80:95], v[168:171], v[136:139], 0\n"
        "v_exp_f32 v96, v96\n"
        "v_exp_f32 v97, v97\n"
        "v_exp_f32 v98, v98\n"
        "v_exp_f32 v99, v99\n"
        "v_cvt_pk_bf16_f32 v112, v96, v97\n"
        "v_cvt_pk_bf16_f32 v113, v98, v99\n"
        "v_mfma_f32_32x32x16_bf16 v[80:95], v[172:175], v[140:143], v[80:95]\n"
        "v_exp_f32 v100, v100\n"
        "v_exp_f32 v101, v101\n"
        "v_exp_f32 v102, v102\n"
        "v_exp_f32 v103, v103\n"
        "v_cvt_pk_bf16_f32 v114, v100, v101\n"
        "v_cvt_pk_bf16_f32 v115, v102, v103\n"
        "v_mfma_f32_32x32x16_bf16 v[16:31], v[204:207], v[116:119], v[16:31]\n"
        "v_exp_f32 v104, v104\n"
        "v_exp_f32 v105, v105\n"
        "v_exp_f32 v106, v106\n"
        "v_exp_f32 v107, v107\n"
        "v_cvt_pk_bf16_f32 v116, v104, v105\n"
        "v_cvt_pk_bf16_f32 v117, v106, v107\n"
        "v_mfma_f32_32x32x16_bf16 v[32:47], v[200:203], v[112:115], v[32:47]\n"
        "v_exp_f32 v108, v108\n"
        "v_exp_f32 v109, v109\n"
        "v_exp_f32 v110, v110\n"
        "v_exp_f32 v111, v111\n"
        "v_cvt_pk_bf16_f32 v118, v108, v109\n"
        "v_cvt_pk_bf16_f32 v119, v110, v111\n"
        "v_mfma_f32_32x32x16_bf16 v[96:111], v[168:171], v[144:147], 0\n"
        "v_exp_f32 v80, v80\n"
        "v_exp_f32 v81, v81\n"
        "v_exp_f32 v82, v82\n"
        "v_exp_f32 v83, v83\n"
        "v_cvt_pk_bf16_f32 v112, v80, v81\n"
        "v_cvt_pk_bf16_f32 v113, v82, v83\n"
        "v_mfma_f32_32x32x16_bf16 v[96:111], v[172:175], v[148:151], v[96:111]\n"
        "v_exp_f32 v84, v84\n"
        "v_exp_f32 v85, v85\n"
        "v_exp_f32 v86, v86\n"
        "v_exp_f32 v87, v87\n"
        "v_cvt_pk_bf16_f32 v114, v84, v85\n"
        "v_cvt_pk_bf16_f32 v115, v86, v87\n"
        "v_mfma_f32_32x32x16_bf16 v[32:47], v[204:207], v[116:119], v[32:47]\n"
        "s_waitcnt vmcnt(4)\n"
        "v_exp_f32 v88, v88\n"
        "v_exp_f32 v89, v89\n"
        "v_exp_f32 v90, v90\n"
        "v_exp_f32 v91, v91\n"
        "v_cvt_pk_bf16_f32 v116, v88, v89\n"
        "v_cvt_pk_bf16_f32 v117, v90, v91\n"
        "v_cndmask_b32 v182, v182, v245, s[16:17]\n"
        "v_cndmask_b32 v183, v183, v246, s[16:17]\n"
        "v_mfma_f32_32x32x16_bf16 v[48:63], v[200:203], v[112:115], v[48:63]\n"
        "v_exp_f32 v92, v92\n"
        "v_exp_f32 v93, v93\n"
        "v_exp_f32 v94, v94\n"
        "v_exp_f32 v95, v95\n"
        "v_cvt_pk_bf16_f32 v118, v92, v93\n"
        "v_cvt_pk_bf16_f32 v119, v94, v95\n"
        "v_mfma_f32_32x32x16_bf16 v[80:95], v[176:179], v[120:123], 0\n"
        "v_exp_f32 v96, v96\n"
        "v_exp_f32 v97, v97\n"
        "v_exp_f32 v98, v98\n"
        "v_exp_f32 v99, v99\n"
        "v_cvt_pk_bf16_f32 v112, v96, v97\n"
        "v_cvt_pk_bf16_f32 v113, v98, v99\n"
        "v_mfma_f32_32x32x16_bf16 v[80:95], v[180:183], v[124:127], v[80:95]\n"
        "v_exp_f32 v100, v100\n"
        "v_exp_f32 v101, v101\n"
        "v_exp_f32 v102, v102\n"
        "v_exp_f32 v103, v103\n"
        "v_cvt_pk_bf16_f32 v114, v100, v101\n"
        "v_cvt_pk_bf16_f32 v115, v102, v103\n"
        "v_mfma_f32_32x32x16_bf16 v[48:63], v[204:207], v[116:119], v[48:63]\n"
        "v_exp_f32 v104, v104\n"
        "v_exp_f32 v105, v105\n"
        "v_exp_f32 v106, v106\n"
        "v_exp_f32 v107, v107\n"
        "v_cvt_pk_bf16_f32 v116, v104, v105\n"
        "v_cvt_pk_bf16_f32 v117, v106, v107\n"
        "v_mfma_f32_32x32x16_bf16 v[64:79], v[200:203], v[112:115], v[64:79]\n"
        "v_exp_f32 v108, v108\n"
        "v_exp_f32 v109, v109\n"
        "v_exp_f32 v110, v110\n"
        "v_exp_f32 v111, v111\n"
        "v_cvt_pk_bf16_f32 v118, v108, v109\n"
        "v_cvt_pk_bf16_f32 v119, v110, v111\n"
        "v_mfma_f32_32x32x16_bf16 v[96:111], v[176:179], v[128:131], 0\n"
        "v_exp_f32 v80, v80\n"
        "v_exp_f32 v81, v81\n"
        "v_exp_f32 v82, v82\n"
        "v_exp_f32 v83, v83\n"
        "v_cvt_pk_bf16_f32 v112, v80, v81\n"
        "v_cvt_pk_bf16_f32 v113, v82, v83\n"
        "v_mfma_f32_32x32x16_bf16 v[96:111], v[180:183], v[132:135], v[96:111]\n"
        "v_exp_f32 v84, v84\n"
        "v_exp_f32 v85, v85\n"
        "v_exp_f32 v86, v86\n"
        "v_exp_f32 v87, v87\n"
        "v_cvt_pk_bf16_f32 v114, v84, v85\n"
        "v_cvt_pk_bf16_f32 v115, v86, v87\n"
        "v_mfma_f32_32x32x16_bf16 v[64:79], v[204:207], v[116:119], v[64:79]\n"
        "v_exp_f32 v88, v88\n"
        "v_exp_f32 v89, v89\n"
        "v_exp_f32 v90, v90\n"
        "v_exp_f32 v91, v91\n"
        "v_cvt_pk_bf16_f32 v116, v88, v89\n"
        "v_cvt_pk_bf16_f32 v117, v90, v91\n"
        "v_mfma_f32_32x32x16_bf16 v[16:31], v[208:211], v[112:115], v[16:31]\n"
        "v_exp_f32 v92, v92\n"
        "v_exp_f32 v93, v93\n"
        "v_exp_f32 v94, v94\n"
        "v_exp_f32 v95, v95\n"
        "v_cvt_pk_bf16_f32 v118, v92, v93\n"
        "v_cvt_pk_bf16_f32 v119, v94, v95\n"
        "v_mfma_f32_32x32x16_bf16 v[80:95], v[176:179], v[136:139], 0\n"
        "v_exp_f32 v96, v96\n"
        "v_exp_f32 v97, v97\n"
        "v_exp_f32 v98, v98\n"
        "v_exp_f32 v99, v99\n"
        "v_cvt_pk_bf16_f32 v112, v96, v97\n"
        "v_cvt_pk_bf16_f32 v113, v98, v99\n"
        "v_mfma_f32_32x32x16_bf16 v[80:95], v[180:183], v[140:143], v[80:95]\n"
        "v_exp_f32 v100, v100\n"
        "v_exp_f32 v101, v101\n"
        "v_exp_f32 v102, v102\n"
        "v_exp_f32 v103, v103\n"
        "v_cvt_pk_bf16_f32 v114, v100, v101\n"
        "v_cvt_pk_bf16_f32 v115, v102, v103\n"
        "v_mfma_f32_32x32x16_bf16 v[16:31], v[212:215], v[116:119], v[16:31]\n"
        "v_exp_f32 v104, v104\n"
        "v_exp_f32 v105, v105\n"
        "v_exp_f32 v106, v106\n"
        "v_exp_f32 v107, v107\n"
        "v_cvt_pk_bf16_f32 v116, v104, v105\n"
        "v_cvt_pk_bf16_f32 v117, v106, v107\n"
        "v_mfma_f32_32x32x16_bf16 v[32:47], v[208:211], v[112:115], v[32:47]\n"
        "v_exp_f32 v108, v108\n"
        "v_exp_f32 v109, v109\n"
        "v_exp_f32 v110, v110\n"
        "v_exp_f32 v111, v111\n"
        "v_cvt_pk_bf16_f32 v118, v108, v109\n"
        "v_cvt_pk_bf16_f32 v119, v110, v111\n"
        "v_mfma_f32_32x32x16_bf16 v[96:111], v[176:179], v[144:147], 0\n"
        "v_exp_f32 v80, v80\n"
        "v_exp_f32 v81, v81\n"
        "v_exp_f32 v82, v82\n"
        "v_exp_f32 v83, v83\n"
        "v_cvt_pk_bf16_f32 v112, v80, v81\n"
        "v_cvt_pk_bf16_f32 v113, v82, v83\n"
        "v_mfma_f32_32x32x16_bf16 v[96:111], v[180:183], v[148:151], v[96:111]\n"
        "v_exp_f32 v84, v84\n"
        "v_exp_f32 v85, v85\n"
        "v_exp_f32 v86, v86\n"
        "v_exp_f32 v87, v87\n"
        "v_cvt_pk_bf16_f32 v114, v84, v85\n"
        "v_cvt_pk_bf16_f32 v115, v86, v87\n"
        "v_mfma_f32_32x32x16_bf16 v[32:47], v[212:215], v[116:119], v[32:47]\n"
        "s_waitcnt vmcnt(0)\n"
        "v_exp_f32 v88, v88\n"
        "v_exp_f32 v89, v89\n"
        "v_exp_f32 v90, v90\n"
        "v_exp_f32 v91, v91\n"
        "v_cvt_pk_bf16_f32 v116, v88, v89\n"
        "v_cvt_pk_bf16_f32 v117, v90, v91\n"
        "v_cndmask_b32 v190, v190, v245, s[16:17]\n"
        "v_cndmask_b32 v191, v191, v246, s[16:17]\n"
        "v_mfma_f32_32x32x16_bf16 v[48:63], v[208:211], v[112:115], v[48:63]\n"
        "v_exp_f32 v92, v92\n"
        "v_exp_f32 v93, v93\n"
        "v_exp_f32 v94, v94\n"
        "v_exp_f32 v95, v95\n"
        "v_cvt_pk_bf16_f32 v118, v92, v93\n"
        "v_cvt_pk_bf16_f32 v119, v94, v95\n"
        "v_mfma_f32_32x32x16_bf16 v[80:95], v[184:187], v[120:123], 0\n"
        "v_exp_f32 v96, v96\n"
        "v_exp_f32 v97, v97\n"
        "v_exp_f32 v98, v98\n"
        "v_exp_f32 v99, v99\n"
        "v_cvt_pk_bf16_f32 v112, v96, v97\n"
        "v_cvt_pk_bf16_f32 v113, v98, v99\n"
        "v_mfma_f32_32x32x16_bf16 v[80:95], v[188:191], v[124:127], v[80:95]\n"
        "v_exp_f32 v100, v100\n"
        "v_exp_f32 v101, v101\n"
        "v_exp_f32 v102, v102\n"
        "v_exp_f32 v103, v103\n"
        "v_cvt_pk_bf16_f32 v114, v100, v101\n"
        "v_cvt_pk_bf16_f32 v115, v102, v103\n"
        "v_mfma_f32_32x32x16_bf16 v[48:63], v[212:215], v[116:119], v[48:63]\n"
        "v_exp_f32 v104, v104\n"
        "v_exp_f32 v105, v105\n"
        "v_exp_f32 v106, v106\n"
        "v_exp_f32 v107, v107\n"
        "v_cvt_pk_bf16_f32 v116, v104, v105\n"
        "v_cvt_pk_bf16_f32 v117, v106, v107\n"
        "v_mfma_f32_32x32x16_bf16 v[64:79], v[208:211], v[112:115], v[64:79]\n"
        "v_exp_f32 v108, v108\n"
        "v_exp_f32 v109, v109\n"
        "v_exp_f32 v110, v110\n"
        "v_exp_f32 v111, v111\n"
        "v_cvt_pk_bf16_f32 v118, v108, v109\n"
        "v_cvt_pk_bf16_f32 v119, v110, v111\n"
        "v_mfma_f32_32x32x16_bf16 v[96:111], v[184:187], v[128:131], 0\n"
        "v_exp_f32 v80, v80\n"
        "v_exp_f32 v81, v81\n"
        "v_exp_f32 v82, v82\n"
        "v_exp_f32 v83, v83\n"
        "v_cvt_pk_bf16_f32 v112, v80, v81\n"
        "v_cvt_pk_bf16_f32 v113, v82, v83\n"
        "v_mfma_f32_32x32x16_bf16 v[96:111], v[188:191], v[132:135], v[96:111]\n"
        "v_exp_f32 v84, v84\n"
        "v_exp_f32 v85, v85\n"
        "v_exp_f32 v86, v86\n"
        "v_exp_f32 v87, v87\n"
        "v_cvt_pk_bf16_f32 v114, v84, v85\n"
        "v_cvt_pk_bf16_f32 v115, v86, v87\n"
        "v_mfma_f32_32x32x16_bf16 v[64:79], v[212:215], v[116:119], v[64:79]\n"
        "v_exp_f32 v88, v88\n"
        "v_exp_f32 v89, v89\n"
        "v_exp_f32 v90, v90\n"
        "v_exp_f32 v91, v91\n"
        "v_cvt_pk_bf16_f32 v116, v88, v89\n"
        "v_cvt_pk_bf16_f32 v117, v90, v91\n"
        "v_mfma_f32_32x32x16_bf16 v[16:31], v[216:219], v[112:115], v[16:31]\n"
        "v_exp_f32 v92, v92\n"
        "v_exp_f32 v93, v93\n"
        "v_exp_f32 v94, v94\n"
        "v_exp_f32 v95, v95\n"
        "v_cvt_pk_bf16_f32 v118, v92, v93\n"
        "v_cvt_pk_bf16_f32 v119, v94, v95\n"
        "v_mfma_f32_32x32x16_bf16 v[80:95], v[184:187], v[136:139], 0\n"
        "v_exp_f32 v96, v96\n"
        "v_exp_f32 v97, v97\n"
        "v_exp_f32 v98, v98\n"
        "v_exp_f32 v99, v99\n"
        "v_cvt_pk_bf16_f32 v112, v96, v97\n"
        "v_cvt_pk_bf16_f32 v113, v98, v99\n"
        "v_mfma_f32_32x32x16_bf16 v[80:95], v[188:191], v[140:143], v[80:95]\n"
        "v_exp_f32 v100, v100\n"
        "v_exp_f32 v101, v101\n"
        "v_exp_f32 v102, v102\n"
        "v_exp_f32 v103, v103\n"
        "v_cvt_pk_bf16_f32 v114, v100, v101\n"
        "v_cvt_pk_bf16_f32 v115, v102, v103\n"
        "v_mfma_f32_32x32x16_bf16 v[16:31], v[220:223], v[116:119], v[16:31]\n"
        "v_exp_f32 v104, v104\n"
        "v_exp_f32 v105, v105\n"
        "v_exp_f32 v106, v106\n"
        "v_exp_f32 v107, v107\n"
        "v_cvt_pk_bf16_f32 v116, v104, v105\n"
        "v_cvt_pk_bf16_f32 v117, v106, v107\n"
        "v_mfma_f32_32x32x16_bf16 v[32:47], v[216:219], v[112:115], v[32:47]\n"
        "v_exp_f32 v108, v108\n"
        "v_exp_f32 v109, v109\n"
        "v_exp_f32 v110, v110\n"
        "v_exp_f32 v111, v111\n"
        "v_cvt_pk_bf16_f32 v118, v108, v109\n"
        "v_cvt_pk_bf16_f32 v119, v110, v111\n"
        "v_mfma_f32_32x32x16_bf16 v[96:111], v[184:187], v[144:147], 0\n"
        "ds_write_b128 v253, v[16:19] offset:0\n"
        "v_exp_f32 v80, v80\n"
        "v_exp_f32 v81, v81\n"
        "v_exp_f32 v82, v82\n"
        "v_exp_f32 v83, v83\n"
        "v_cvt_pk_bf16_f32 v112, v80, v81\n"
        "v_cvt_pk_bf16_f32 v113, v82, v83\n"
        "v_mfma_f32_32x32x16_bf16 v[96:111], v[188:191], v[148:151], v[96:111]\n"
        "ds_write_b128 v253, v[20:23] offset:1024\n"
        "v_exp_f32 v84, v84\n"
        "v_exp_f32 v85, v85\n"
        "v_exp_f32 v86, v86\n"
        "v_exp_f32 v87, v87\n"
        "v_cvt_pk_bf16_f32 v114, v84, v85\n"
        "v_cvt_pk_bf16_f32 v115, v86, v87\n"
        "v_mfma_f32_32x32x16_bf16 v[32:47], v[220:223], v[116:119], v[32:47]\n"
        "ds_write_b128 v253, v[24:27] offset:2048\n"
        "v_exp_f32 v88, v88\n"
        "v_exp_f32 v89, v89\n"
        "v_exp_f32 v90, v90\n"
        "v_exp_f32 v91, v91\n"
        "v_cvt_pk_bf16_f32 v116, v88, v89\n"
        "v_cvt_pk_bf16_f32 v117, v90, v91\n"
        "v_mfma_f32_32x32x16_bf16 v[48:63], v[216:219], v[112:115], v[48:63]\n"
        "ds_write_b128 v253, v[28:31] offset:3072\n"
        "v_exp_f32 v92, v92\n"
        "v_exp_f32 v93, v93\n"
        "v_exp_f32 v94, v94\n"
        "v_exp_f32 v95, v95\n"
        "v_cvt_pk_bf16_f32 v118, v92, v93\n"
        "v_cvt_pk_bf16_f32 v119, v94, v95\n"
        "ds_write_b128 v254, v[32:35] offset:0\n"
        "v_exp_f32 v96, v96\n"
        "v_exp_f32 v97, v97\n"
        "v_exp_f32 v98, v98\n"
        "v_exp_f32 v99, v99\n"
        "v_cvt_pk_bf16_f32 v112, v96, v97\n"
        "v_cvt_pk_bf16_f32 v113, v98, v99\n"
        "ds_write_b128 v254, v[36:39] offset:1024\n"
        "v_exp_f32 v100, v100\n"
        "v_exp_f32 v101, v101\n"
        "v_exp_f32 v102, v102\n"
        "v_exp_f32 v103, v103\n"
        "v_cvt_pk_bf16_f32 v114, v100, v101\n"
        "v_cvt_pk_bf16_f32 v115, v102, v103\n"
        "v_mfma_f32_32x32x16_bf16 v[48:63], v[220:223], v[116:119], v[48:63]\n"
        "ds_write_b128 v254, v[40:43] offset:2048\n"
        "v_exp_f32 v104, v104\n"
        "v_exp_f32 v105, v105\n"
        "v_exp_f32 v106, v106\n"
        "v_exp_f32 v107, v107\n"
        "v_cvt_pk_bf16_f32 v116, v104, v105\n"
        "v_cvt_pk_bf16_f32 v117, v106, v107\n"
        "v_mfma_f32_32x32x16_bf16 v[64:79], v[216:219], v[112:115], v[64:79]\n"
        "ds_write_b128 v254, v[44:47] offset:3072\n"
        "v_exp_f32 v108, v108\n"
        "v_exp_f32 v109, v109\n"
        "v_exp_f32 v110, v110\n"
        "v_exp_f32 v111, v111\n"
        "v_cvt_pk_bf16_f32 v118, v108, v109\n"
        "v_cvt_pk_bf16_f32 v119, v110, v111\n"
        "s_setprio 2\n"
        "ds_write_b128 v255, v[48:51] offset:0\n"
        "v_mfma_f32_32x32x16_bf16 v[64:79], v[220:223], v[116:119], v[64:79]\n"
        "ds_write_b128 v255, v[52:55] offset:1024\n"
        "ds_write_b128 v255, v[56:59] offset:2048\n"
        "ds_write_b128 v255, v[60:63] offset:3072\n"
        "s_add_u32 s14, s14, 4\n"
        "s_mov_b64 exec, 1\n"
        "ds_add_u32 v241, v242\n"
        "s_mov_b64 exec, -1\n"
        "s_mov_b32 s26, 0\n"
        ".Lspin_6:\n"
        "ds_read_b32 v1, v241\n"
        "s_waitcnt lgkmcnt(0)\n"
        ".Lsmid_8:\n"
        "v_readfirstlane_b32 s27, v1\n"
        "s_add_u32 s26, s26, 1\n"
        "s_and_b32 s25, s27, 0xffff\n"
        "s_cmp_ge_u32 s25, s14\n"
        "s_cbranch_scc1 .Lsdone_7\n"
        "s_cmp_lt_u32 s26, 400000\n"
        "s_cbranch_scc1 .Lspin_6\n"
        ".Lsdone_7:\n"
        "ds_read_b128 v[16:19], v239 offset:0\n"
        "ds_read_b128 v[20:23], v239 offset:1024\n"
        "ds_read_b128 v[24:27], v239 offset:2048\n"
        "ds_read_b128 v[28:31], v239 offset:3072\n"
        "ds_read_b128 v[32:35], v239 offset:4096\n"
        "ds_read_b128 v[36:39], v239 offset:5120\n"
        "ds_read_b128 v[40:43], v239 offset:6144\n"
        "ds_read_b128 v[44:47], v239 offset:7168\n"
        "ds_read_b128 v[48:51], v239 offset:8192\n"
        "ds_read_b128 v[52:55], v239 offset:9216\n"
        "ds_read_b128 v[56:59], v239 offset:10240\n"
        "ds_read_b128 v[60:63], v239 offset:11264\n"
        "s_waitcnt lgkmcnt(8)\n"
        "v_pk_add_f32 v[64:65], v[64:65], v[16:17]\n"
        "v_pk_add_f32 v[66:67], v[66:67], v[18:19]\n"
        "v_pk_add_f32 v[68:69], v[68:69], v[20:21]\n"
        "v_pk_add_f32 v[70:71], v[70:71], v[22:23]\n"
        "v_pk_add_f32 v[72:73], v[72:73], v[24:25]\n"
        "v_pk_add_f32 v[74:75], v[74:75], v[26:27]\n"
        "v_pk_add_f32 v[76:77], v[76:77], v[28:29]\n"
        "v_pk_add_f32 v[78:79], v[78:79], v[30:31]\n"
        "s_waitcnt lgkmcnt(4)\n"
        "v_pk_add_f32 v[64:65], v[64:65], v[32:33]\n"
        "v_pk_add_f32 v[66:67], v[66:67], v[34:35]\n"
        "v_pk_add_f32 v[68:69], v[68:69], v[36:37]\n"
        "v_pk_add_f32 v[70:71], v[70:71], v[38:39]\n"
        "v_pk_add_f32 v[72:73], v[72:73], v[40:41]\n"
        "v_pk_add_f32 v[74:75], v[74:75], v[42:43]\n"
        "v_pk_add_f32 v[76:77], v[76:77], v[44:45]\n"
        "v_pk_add_f32 v[78:79], v[78:79], v[46:47]\n"
        "s_waitcnt lgkmcnt(0)\n"
        "v_pk_add_f32 v[64:65], v[64:65], v[48:49]\n"
        "v_pk_add_f32 v[66:67], v[66:67], v[50:51]\n"
        "v_pk_add_f32 v[68:69], v[68:69], v[52:53]\n"
        "v_pk_add_f32 v[70:71], v[70:71], v[54:55]\n"
        "v_pk_add_f32 v[72:73], v[72:73], v[56:57]\n"
        "v_pk_add_f32 v[74:75], v[74:75], v[58:59]\n"
        "v_pk_add_f32 v[76:77], v[76:77], v[60:61]\n"
        "v_pk_add_f32 v[78:79], v[78:79], v[62:63]\n"
        "v_mov_b32 v1, v76\n"
        "v_mov_b32 v2, v76\n"
        "v_mov_b32 v6, 0\n"
        "v_mov_b32 v7, 0\n"
        "v_permlane32_swap_b32 v1, v2\n"
        "v_mov_b32 v3, 0x71800000\n"
        "v_rcp_f32 v4, v2\n"
        "v_cmp_nlt_f32 s[20:21], v2, v3\n"
        "v_pk_mul_f32 v[64:65], v[64:65], v[4:5] op_sel_hi:[1,0]\n"
        "v_pk_mul_f32 v[66:67], v[66:67], v[4:5] op_sel_hi:[1,0]\n"
        "v_pk_mul_f32 v[68:69], v[68:69], v[4:5] op_sel_hi:[1,0]\n"
        "v_pk_mul_f32 v[70:71], v[70:71], v[4:5] op_sel_hi:[1,0]\n"
        "v_pk_mul_f32 v[72:73], v[72:73], v[4:5] op_sel_hi:[1,0]\n"
        "v_pk_mul_f32 v[74:75], v[74:75], v[4:5] op_sel_hi:[1,0]\n"
        "v_pk_mul_f32 v[76:77], v[76:77], v[4:5] op_sel_hi:[1,0]\n"
        "v_pk_mul_f32 v[78:79], v[78:79], v[4:5] op_sel_hi:[1,0]\n"
        "v_cvt_pk_bf16_f32 v80, v64, v65\n"
        "v_cvt_pk_bf16_f32 v81, v66, v67\n"
        "v_cvt_pk_bf16_f32 v82, v68, v69\n"
        "v_cvt_pk_bf16_f32 v83, v70, v71\n"
        "v_cvt_pk_bf16_f32 v84, v72, v73\n"
        "v_cvt_pk_bf16_f32 v85, v74, v75\n"
        "v_cvt_pk_bf16_f32 v86, v76, v77\n"
        "v_cvt_pk_bf16_f32 v87, v78, v79\n"
        "s_mul_i32 s18, s72, 28672\n"
        "s_add_u32 s18, s18, 4194304\n"
        "v_mov_b32 v1, 1073741824\n"
        "v_cndmask_b32 v2, v244, v1, s[16:17]\n"
        "v_cndmask_b32 v3, v1, v244, s[16:17]\n"
        "buffer_store_dwordx4 v[80:83], v243, s[64:67], s18 offen sc1\n"
        "buffer_store_dwordx4 v[84:87], v2, s[64:67], s18 offen sc1\n"
        "buffer_store_dwordx2 v[84:85], v3, s[64:67], s18 offen sc1\n"
        "v_pk_fma_f32 v[6:7], v[64:65], 0, v[6:7]\n"
        "v_pk_fma_f32 v[6:7], v[66:67], 0, v[6:7]\n"
        "v_pk_fma_f32 v[6:7], v[68:69], 0, v[6:7]\n"
        "v_pk_fma_f32 v[6:7], v[70:71], 0, v[6:7]\n"
        "v_pk_fma_f32 v[6:7], v[72:73], 0, v[6:7]\n"
        "v_pk_fma_f32 v[6:7], v[74:75], 0, v[6:7]\n"
        "v_pk_fma_f32 v[6:7], v[76:77], 0, v[6:7]\n"
        "v_pk_fma_f32 v[6:7], v[78:79], 0, v[6:7]\n"
        "v_cmp_u_f32 vcc, v6, v7\n"
        "s_or_b64 vcc, vcc, s[20:21]\n"
        "s_cselect_b32 s18, 0x10001, 1\n"
        "v_mov_b32 v8, s18\n"
        "s_add_u32 s14, s14, 4\n"
        "s_lshl_b32 s19, s8, 4\n"
        "s_add_u32 s19, s19, s9\n"
        "s_lshl_b32 s19, s19, 7\n"
        "s_add_u32 s19, s19, 69632\n"
        "s_waitcnt vmcnt(0)\n"
        "s_mov_b64 exec, 1\n"
        "ds_add_rtn_u32 v1, v241, v8\n"
        "s_mov_b64 exec, -1\n"
        "s_waitcnt lgkmcnt(0)\n"
        "v_readfirstlane_b32 s27, v1\n"
        "s_add_u32 s27, s27, s18\n"
        "s_and_b32 s20, s27, 0xffff\n"
        "s_cmp_lg_u32 s20, s14\n"
        "s_cbranch_scc1 .LA_notlast\n"
        "s_lshr_b32 s27, s27, 16\n"
        "s_cmp_eq_u32 s27, 0\n"
        "s_cbranch_scc1 .LA_pub\n"
        "s_cmp_eq_u32 s76, 0\n"
        "s_cbranch_scc1 .LA_fallback\n"
        ".LA_pub:\n"
        "v_mov_b32 v2, s68\n"
        "v_mov_b32 v3, s69\n"
        "s_mov_b64 exec, 1\n"
        "buffer_store_dwordx2 v[2:3], off, s[64:67], s19 sc1\n"
        "s_mov_b64 exec, -1\n"
        "s_or_b32 s18, s8, s9\n"
        "s_cmp_lg_u32 s18, 0\n"
        "s_cbranch_scc1 .LA_end\n"
        "s_setprio 0\n"
        "v_lshlrev_b32 v4, 7, v0\n"
        "v_add_u32 v4, 69632, v4\n"
        "s_mov_b32 s19, 0\n"
        "s_mov_b32 s26, 0\n"
        ".Lpoll_9:\n"
        "buffer_load_dwordx2 v[6:7], v4, s[64:67], s19 offen sc1\n"
        "s_add_u32 s26, s26, 1\n"
        "s_waitcnt vmcnt(0)\n"
        "v_cmp_ne_u64 vcc, s[68:69], v[6:7]\n"
        "s_cbranch_vccz .Lpdone_10\n"
        "s_sleep 8\n"
        "s_cmp_lt_u32 s26, 50000\n"
        "s_cbranch_scc1 .Lpoll_9\n"
        ".Lpdone_10:\n"
        "s_mov_b32 s19, 8192\n"
        "s_mov_b32 s26, 0\n"
        ".Lpoll_11:\n"
        "buffer_load_dwordx2 v[6:7], v4, s[64:67], s19 offen sc1\n"
        "s_add_u32 s26, s26, 1\n"
        "s_waitcnt vmcnt(0)\n"
        "v_cmp_ne_u64 vcc, s[68:69], v[6:7]\n"
        "s_cbranch_vccz .Lpdone_12\n"
        "s_sleep 8\n"
        "s_cmp_lt_u32 s26, 50000\n"
        "s_cbranch_scc1 .Lpoll_11\n"
        ".Lpdone_12:\n"
        "s_mov_b32 s19, 16384\n"
        "s_mov_b32 s26, 0\n"
        ".Lpoll_13:\n"
        "buffer_load_dwordx2 v[6:7], v4, s[64:67], s19 offen sc1\n"
        "s_add_u32 s26, s26, 1\n"
        "s_waitcnt vmcnt(0)\n"
        "v_cmp_ne_u64 vcc, s[68:69], v[6:7]\n"
        "s_cbranch_vccz .Lpdone_14\n"
        "s_sleep 8\n"
        "s_cmp_lt_u32 s26, 50000\n"
        "s_cbranch_scc1 .Lpoll_13\n"
        ".Lpdone_14:\n"
        "s_mov_b32 s19, 24576\n"
        "s_mov_b32 s26, 0\n"
        ".Lpoll_15:\n"
        "buffer_load_dwordx2 v[6:7], v4, s[64:67], s19 offen sc1\n"
        "s_add_u32 s26, s26, 1\n"
        "s_waitcnt vmcnt(0)\n"
        "v_cmp_ne_u64 vcc, s[68:69], v[6:7]\n"
        "s_cbranch_vccz .Lpdone_16\n"
        "s_sleep 8\n"
        "s_cmp_lt_u32 s26, 50000\n"
        "s_cbranch_scc1 .Lpoll_15\n"
        ".Lpdone_16:\n"
        "s_mov_b32 s18, 0\n"
        "v_mov_b32 v2, s68\n"
        "v_mov_b32 v3, s69\n"
        "s_mov_b64 exec, 1\n"
        "buffer_store_dwordx2 v[2:3], off, s[64:67], s18 sc1\n"
        "s_mov_b64 exec, -1\n"
        ".LA_end:\n"
        "s_endpgm\n"
        ".LA_notlast:\n"
        "s_setprio 0\n"
        "s_mov_b32 s26, 0\n"
        ".Lspin_17:\n"
        "ds_read_b32 v1, v241\n"
        "s_waitcnt lgkmcnt(0)\n"
        ".Lsmid_19:\n"
        "v_readfirstlane_b32 s27, v1\n"
        "s_add_u32 s26, s26, 1\n"
        "s_and_b32 s25, s27, 0xffff\n"
        "s_cmp_ge_u32 s25, s14\n"
        "s_cbranch_scc1 .Lsdone_18\n"
        "s_cmp_lt_u32 s26, 400000\n"
        "s_cbranch_scc1 .Lspin_17\n"
        ".Lsdone_18:\n"
        "s_lshr_b32 s27, s27, 16\n"
        "s_cmp_eq_u32 s27, 0\n"
        "s_cbranch_scc1 .LA_end\n"
        "s_cmp_eq_u32 s76, 0\n"
        "s_cbranch_scc0 .LA_end\n"
        ".LA_fallback:\n"
        "s_mov_b32 s76, 1\n"
        "s_mov_b32 s77, 0\n"
        "s_mov_b32 s75, 0\n"
        "v_cndmask_b32 v126, v126, 0, s[16:17]\n"
        "v_cndmask_b32 v127, v127, 0, s[16:17]\n"
        "v_mov_b32 v232, 0xff800000\n"
        "v_cndmask_b32 v134, v134, 0, s[16:17]\n"
        "v_cndmask_b32 v135, v135, 0, s[16:17]\n"
        "v_mov_b32 v233, 0xff800000\n"
        "v_cndmask_b32 v142, v142, 0, s[16:17]\n"
        "v_cndmask_b32 v143, v143, 0, s[16:17]\n"
        "v_mov_b32 v234, 0xff800000\n"
        "v_cndmask_b32 v150, v150, 0, s[16:17]\n"
        "v_cndmask_b32 v151, v151, 0, s[16:17]\n"
        "v_mov_b32 v235, 0xff800000\n"
        "s_mul_i32 s18, s10, 458752\n"
        "s_add_u32 s18, s18, 131072\n"
        "s_add_u32 s96, s64, s18\n"
        "s_addc_u32 s97, s65, 0\n"
        "s_lshl_b32 s19, s8, 17\n"
        "s_lshl_b32 s18, s10, 15\n"
        "s_add_u32 s18, s18, s19\n"
        "s_add_u32 s18, s18, 2097152\n"
        "s_add_u32 s98, s64, s18\n"
        "s_addc_u32 s99, s65, 0\n"
        ".LA_fbl:\n"
        "global_load_dwordx4 v[160:163], v243, s[96:97] sc1\n"
        "global_load_dwordx4 v[164:167], v244, s[96:97] sc1\n"
        "s_add_u32 s96, s96, 28672\n"
        "s_addc_u32 s97, s97, 0\n"
        "s_add_u32 s75, s75, 1\n"
        "s_waitcnt vmcnt(0)\n"
        "v_cndmask_b32 v166, v166, v245, s[16:17]\n"
        "v_cndmask_b32 v167, v167, v246, s[16:17]\n"
        "s_nop 1\n"
        "v_mfma_f32_32x32x16_bf16 v[80:95], v[160:163], v[120:123], 0\n"
        "v_mfma_f32_32x32x16_bf16 v[80:95], v[164:167], v[124:127], v[80:95]\n"
        "s_nop 11\n"
        "v_max3_f32 v232, v232, v80, v81\n"
        "v_max3_f32 v232, v232, v82, v83\n"
        "v_max3_f32 v232, v232, v84, v85\n"
        "v_max3_f32 v232, v232, v86, v87\n"
        "v_max3_f32 v232, v232, v88, v89\n"
        "v_max3_f32 v232, v232, v90, v91\n"
        "v_max3_f32 v232, v232, v92, v93\n"
        "v_max3_f32 v232, v232, v94, v95\n"
        "v_mfma_f32_32x32x16_bf16 v[80:95], v[160:163], v[128:131], 0\n"
        "v_mfma_f32_32x32x16_bf16 v[80:95], v[164:167], v[132:135], v[80:95]\n"
        "s_nop 11\n"
        "v_max3_f32 v233, v233, v80, v81\n"
        "v_max3_f32 v233, v233, v82, v83\n"
        "v_max3_f32 v233, v233, v84, v85\n"
        "v_max3_f32 v233, v233, v86, v87\n"
        "v_max3_f32 v233, v233, v88, v89\n"
        "v_max3_f32 v233, v233, v90, v91\n"
        "v_max3_f32 v233, v233, v92, v93\n"
        "v_max3_f32 v233, v233, v94, v95\n"
        "v_mfma_f32_32x32x16_bf16 v[80:95], v[160:163], v[136:139], 0\n"
        "v_mfma_f32_32x32x16_bf16 v[80:95], v[164:167], v[140:143], v[80:95]\n"
        "s_nop 11\n"
        "v_max3_f32 v234, v234, v80, v81\n"
        "v_max3_f32 v234, v234, v82, v83\n"
        "v_max3_f32 v234, v234, v84, v85\n"
        "v_max3_f32 v234, v234, v86, v87\n"
        "v_max3_f32 v234, v234, v88, v89\n"
        "v_max3_f32 v234, v234, v90, v91\n"
        "v_max3_f32 v234, v234, v92, v93\n"
        "v_max3_f32 v234, v234, v94, v95\n"
        "v_mfma_f32_32x32x16_bf16 v[80:95], v[160:163], v[144:147], 0\n"
        "v_mfma_f32_32x32x16_bf16 v[80:95], v[164:167], v[148:151], v[80:95]\n"
        "s_nop 11\n"
        "v_max3_f32 v235, v235, v80, v81\n"
        "v_max3_f32 v235, v235, v82, v83\n"
        "v_max3_f32 v235, v235, v84, v85\n"
        "v_max3_f32 v235, v235, v86, v87\n"
        "v_max3_f32 v235, v235, v88, v89\n"
        "v_max3_f32 v235, v235, v90, v91\n"
        "v_max3_f32 v235, v235, v92, v93\n"
        "v_max3_f32 v235, v235, v94, v95\n"
        "s_cmp_lt_u32 s75, 16\n"
        "s_cbranch_scc1 .LA_fbl\n"
        "v_mov_b32 v4, v232\n"
        "v_mov_b32 v5, v233\n"
        "v_mov_b32 v6, v234\n"
        "v_mov_b32 v7, v235\n"
        "s_nop 0\n"
        "v_permlane32_swap_b32 v4, v232\n"
        "v_permlane32_swap_b32 v5, v233\n"
        "v_permlane32_swap_b32 v6, v234\n"
        "v_permlane32_swap_b32 v7, v235\n"
        "v_and_b32 v9, 31, v0\n"
        "v_lshlrev_b32 v9, 4, v9\n"
        "v_max_f32 v232, v232, v4\n"
        "s_add_u32 s18, s10, 1\n"
        "s_and_b32 s18, s18, 3\n"
        "s_lshl_b32 s18, s18, 9\n"
        "s_add_u32 s18, s18, 160768\n"
        "v_add_u32 v10, s18, v9\n"
        "s_lshl_b32 s18, s10, 2\n"
        "v_add_u32 v1, s18, v10\n"
        "ds_write_b32 v1, v232\n"
        "v_max_f32 v233, v233, v5\n"
        "s_add_u32 s18, s10, 2\n"
        "s_and_b32 s18, s18, 3\n"
        "s_lshl_b32 s18, s18, 9\n"
        "s_add_u32 s18, s18, 160768\n"
        "v_add_u32 v11, s18, v9\n"
        "s_lshl_b32 s18, s10, 2\n"
        "v_add_u32 v1, s18, v11\n"
        "ds_write_b32 v1, v233\n"
        "v_max_f32 v234, v234, v6\n"
        "s_add_u32 s18, s10, 3\n"
        "s_and_b32 s18, s18, 3\n"
        "s_lshl_b32 s18, s18, 9\n"
        "s_add_u32 s18, s18, 160768\n"
        "v_add_u32 v12, s18, v9\n"
        "s_lshl_b32 s18, s10, 2\n"
        "v_add_u32 v1, s18, v12\n"
        "ds_write_b32 v1, v234\n"
        "v_max_f32 v235, v235, v7\n"
        "s_add_u32 s18, s10, 4\n"
        "s_and_b32 s18, s18, 3\n"
        "s_lshl_b32 s18, s18, 9\n"
        "s_add_u32 s18, s18, 160768\n"
        "v_add_u32 v13, s18, v9\n"
        "s_lshl_b32 s18, s10, 2\n"
        "v_add_u32 v1, s18, v13\n"
        "ds_write_b32 v1, v235\n"
        "s_add_u32 s14, s14, 4\n"
        "s_mov_b64 exec, 1\n"
        "ds_add_u32 v241, v242\n"
        "s_mov_b64 exec, -1\n"
        "s_mov_b32 s26, 0\n"
        ".Lspin_20:\n"
        "ds_read_b32 v1, v241\n"
        "s_waitcnt lgkmcnt(0)\n"
        ".Lsmid_22:\n"
        "v_readfirstlane_b32 s27, v1\n"
        "s_add_u32 s26, s26, 1\n"
        "s_and_b32 s25, s27, 0xffff\n"
        "s_cmp_ge_u32 s25, s14\n"
        "s_cbranch_scc1 .Lsdone_21\n"
        "s_cmp_lt_u32 s26, 400000\n"
        "s_cbranch_scc1 .Lspin_20\n"
        ".Lsdone_21:\n"
        "ds_read_b128 v[80:83], v10\n"
        "ds_read_b128 v[84:87], v11\n"
        "ds_read_b128 v[88:91], v12\n"
        "ds_read_b128 v[92:95], v13\n"
        "v_mov_b32 v16, 0\n"
        "v_mov_b32 v17, 0\n"
        "v_mov_b32 v18, 0\n"
        "v_mov_b32 v19, 0\n"
        "v_mov_b32 v20, 0\n"
        "v_mov_b32 v21, 0\n"
        "v_mov_b32 v22, 0\n"
        "v_mov_b32 v23, 0\n"
        "v_mov_b32 v24, 0\n"
        "v_mov_b32 v25, 0\n"
        "v_mov_b32 v26, 0\n"
        "v_mov_b32 v27, 0\n"
        "v_mov_b32 v28, 0\n"
        "v_mov_b32 v29, 0\n"
        "v_mov_b32 v30, 0\n"
        "v_mov_b32 v31, 0\n"
        "v_mov_b32 v32, 0\n"
        "v_mov_b32 v33, 0\n"
        "v_mov_b32 v34, 0\n"
        "v_mov_b32 v35, 0\n"
        "v_mov_b32 v36, 0\n"
        "v_mov_b32 v37, 0\n"
        "v_mov_b32 v38, 0\n"
        "v_mov_b32 v39, 0\n"
        "v_mov_b32 v40, 0\n"
        "v_mov_b32 v41, 0\n"
        "v_mov_b32 v42, 0\n"
        "v_mov_b32 v43, 0\n"
        "v_mov_b32 v44, 0\n"
        "v_mov_b32 v45, 0\n"
        "v_mov_b32 v46, 0\n"
        "v_mov_b32 v47, 0\n"
        "v_mov_b32 v48, 0\n"
        "v_mov_b32 v49, 0\n"
        "v_mov_b32 v50, 0\n"
        "v_mov_b32 v51, 0\n"
        "v_mov_b32 v52, 0\n"
        "v_mov_b32 v53, 0\n"
        "v_mov_b32 v54, 0\n"
        "v_mov_b32 v55, 0\n"
        "v_mov_b32 v56, 0\n"
        "v_mov_b32 v57, 0\n"
        "v_mov_b32 v58, 0\n"
        "v_mov_b32 v59, 0\n"
        "v_mov_b32 v60, 0\n"
        "v_mov_b32 v61, 0\n"
        "v_mov_b32 v62, 0\n"
        "v_mov_b32 v63, 0\n"
        "v_mov_b32 v64, 0\n"
        "v_mov_b32 v65, 0\n"
        "v_mov_b32 v66, 0\n"
        "v_mov_b32 v67, 0\n"
        "v_mov_b32 v68, 0\n"
        "v_mov_b32 v69, 0\n"
        "v_mov_b32 v70, 0\n"
        "v_mov_b32 v71, 0\n"
        "v_mov_b32 v72, 0\n"
        "v_mov_b32 v73, 0\n"
        "v_mov_b32 v74, 0\n"
        "v_mov_b32 v75, 0\n"
        "v_mov_b32 v76, 0\n"
        "v_mov_b32 v77, 0\n"
        "v_mov_b32 v78, 0\n"
        "v_mov_b32 v79, 0\n"
        "v_mov_b32 v112, 0\n"
        "v_mov_b32 v113, 0\n"
        "v_mov_b32 v114, 0\n"
        "v_mov_b32 v115, 0\n"
        "v_mov_b32 v116, 0\n"
        "v_mov_b32 v117, 0\n"
        "v_mov_b32 v118, 0\n"
        "v_mov_b32 v119, 0\n"
        "v_mov_b32 v220, 0\n"
        "v_mov_b32 v221, 0\n"
        "v_mov_b32 v222, 0\n"
        "v_mov_b32 v223, 0\n"
        "s_mul_i32 s18, s10, 458752\n"
        "s_add_u32 s18, s18, 131072\n"
        "s_add_u32 s96, s64, s18\n"
        "s_addc_u32 s97, s65, 0\n"
        "s_lshl_b32 s19, s8, 17\n"
        "s_lshl_b32 s18, s10, 15\n"
        "s_add_u32 s18, s18, s19\n"
        "s_add_u32 s18, s18, 2097152\n"
        "s_add_u32 s98, s64, s18\n"
        "s_addc_u32 s99, s65, 0\n"
        "global_load_dwordx4 v[160:163], v243, s[96:97] sc1\n"
        "global_load_dwordx4 v[164:167], v244, s[96:97] sc1\n"
        "global_load_dwordx4 v[192:195], v240, s[98:99] offset:0 sc1\n"
        "global_load_dwordx4 v[196:199], v240, s[98:99] offset:1024 sc1\n"
        "s_add_u32 s96, s96, 28672\n"
        "s_addc_u32 s97, s97, 0\n"
        "s_add_u32 s98, s98, 2048\n"
        "s_addc_u32 s99, s99, 0\n"
        "global_load_dwordx4 v[168:171], v243, s[96:97] sc1\n"
        "global_load_dwordx4 v[172:175], v244, s[96:97] sc1\n"
        "global_load_dwordx4 v[200:203], v240, s[98:99] offset:0 sc1\n"
        "global_load_dwordx4 v[204:207], v240, s[98:99] offset:1024 sc1\n"
        "s_add_u32 s96, s96, 28672\n"
        "s_addc_u32 s97, s97, 0\n"
        "s_add_u32 s98, s98, 2048\n"
        "s_addc_u32 s99, s99, 0\n"
        "global_load_dwordx4 v[176:179], v243, s[96:97] sc1\n"
        "global_load_dwordx4 v[180:183], v244, s[96:97] sc1\n"
        "global_load_dwordx4 v[208:211], v240, s[98:99] offset:0 sc1\n"
        "global_load_dwordx4 v[212:215], v240, s[98:99] offset:1024 sc1\n"
        "s_add_u32 s96, s96, 28672\n"
        "s_addc_u32 s97, s97, 0\n"
        "s_waitcnt lgkmcnt(0)\n"
        "v_max3_f32 v232, v80, v81, v82\n"
        "v_max_f32 v232, v232, v83\n"
        "v_max3_f32 v233, v84, v85, v86\n"
        "v_max_f32 v233, v233, v87\n"
        "v_max3_f32 v234, v88, v89, v90\n"
        "v_max_f32 v234, v234, v91\n"
        "v_max3_f32 v235, v92, v93, v94\n"
        "v_max_f32 v235, v235, v95\n"
        "s_branch .LA_ref\n"
        ".LG:\n"
        "v_lshrrev_b32 v1, 3, v0\n"
        "v_mul_u32_u24 v1, 896, v1\n"
        "v_lshrrev_b32 v2, 4, v0\n"
        "v_xor_b32 v2, v2, v0\n"
        "v_and_b32 v2, 7, v2\n"
        "v_lshlrev_b32 v2, 4, v2\n"
        "v_xor_b32 v3, 64, v2\n"
        "s_lshl_b32 s72, s9, 2\n"
        "s_add_u32 s72, s72, s10\n"
        "s_mul_i32 s18, s72, 28672\n"
        "v_add_u32 v1, s18, v1\n"
        "v_add_u32 v232, v1, v2\n"
        "v_add_u32 v233, v1, v3\n"
        "v_add_u32 v233, 7168, v233\n"
        "v_add_u32 v234, 14336, v232\n"
        "v_add_u32 v235, 14336, v233\n"
        "s_lshl_b32 s70, s10, 12\n"
        "s_waitcnt lgkmcnt(0)\n"
        "s_add_u32 m0, s70, 0\n"
        "s_lshl_b32 s71, s11, 7\n"
        "buffer_load_dwordx4 v232, s[32:35], s71 offen  lds\n"
        "s_add_u32 m0, s70, 1024\n"
        "s_nop 0\n"
        "buffer_load_dwordx4 v233, s[32:35], s71 offen  lds\n"
        "s_add_u32 m0, s70, 2048\n"
        "s_nop 0\n"
        "buffer_load_dwordx4 v234, s[32:35], s71 offen  lds\n"
        "s_add_u32 m0, s70, 3072\n"
        "s_nop 0\n"
        "buffer_load_dwordx4 v235, s[32:35], s71 offen  lds\n"
        "s_add_u32 s90, s11, 1\n"
        "s_cmp_eq_u32 s90, 7\n"
        "s_cselect_b32 s90, 0, s90\n"
        "s_add_u32 s91, s90, 1\n"
        "s_cmp_eq_u32 s91, 7\n"
        "s_cselect_b32 s91, 0, s91\n"
        "s_add_u32 s92, s91, 1\n"
        "s_cmp_eq_u32 s92, 7\n"
        "s_cselect_b32 s92, 0, s92\n"
        "s_add_u32 s93, s92, 1\n"
        "s_cmp_eq_u32 s93, 7\n"
        "s_cselect_b32 s93, 0, s93\n"
        "s_add_u32 s94, s93, 1\n"
        "s_cmp_eq_u32 s94, 7\n"
        "s_cselect_b32 s94, 0, s94\n"
        "s_add_u32 s95, s94, 1\n"
        "s_cmp_eq_u32 s95, 7\n"
        "s_cselect_b32 s95, 0, s95\n"
        "s_add_u32 m0, s70, 16384\n"
        "s_lshl_b32 s71, s90, 7\n"
        "buffer_load_dwordx4 v232, s[32:35], s71 offen  lds\n"
        "s_add_u32 m0, s70, 17408\n"
        "s_nop 0\n"
        "buffer_load_dwordx4 v233, s[32:35], s71 offen  lds\n"
        "s_add_u32 m0, s70, 18432\n"
        "s_nop 0\n"
        "buffer_load_dwordx4 v234, s[32:35], s71 offen  lds\n"
        "s_add_u32 m0, s70, 19456\n"
        "s_nop 0\n"
        "buffer_load_dwordx4 v235, s[32:35], s71 offen  lds\n"
        "s_add_u32 m0, s70, 32768\n"
        "s_lshl_b32 s71, s91, 7\n"
        "buffer_load_dwordx4 v232, s[32:35], s71 offen  lds\n"
        "s_add_u32 m0, s70, 33792\n"
        "s_nop 0\n"
        "buffer_load_dwordx4 v233, s[32:35], s71 offen  lds\n"
        "s_add_u32 m0, s70, 34816\n"
        "s_nop 0\n"
        "buffer_load_dwordx4 v234, s[32:35], s71 offen  lds\n"
        "s_add_u32 m0, s70, 35840\n"
        "s_nop 0\n"
        "buffer_load_dwordx4 v235, s[32:35], s71 offen  lds\n"
        "s_load_dwordx2 s[68:69], s[64:65], 0x0\n"
        "v_and_b32 v1, 31, v0\n"
        "v_bfe_u32 v2, v0, 1, 3\n"
        "v_lshrrev_b32 v236, 5, v0\n"
        "v_xor_b32 v2, v2, v236\n"
        "v_lshlrev_b32 v2, 4, v2\n"
        "v_lshl_or_b32 v236, v1, 7, v2\n"
        "s_lshl_b32 s18, s10, 12\n"
        "v_add_u32 v236, s18, v236\n"
        "v_xor_b32 v237, 32, v236\n"
        "v_xor_b32 v238, 64, v236\n"
        "v_xor_b32 v239, 0x60, v236\n"
        "v_and_b32 v248, 16, v0\n"
        "v_lshlrev_b32 v248, 1, v248\n"
        "v_and_b32 v1, 1, v0\n"
        "v_lshl_or_b32 v248, v1, 4, v248\n"
        "v_and_b32 v1, 2, v0\n"
        "v_lshl_or_b32 v248, v1, 2, v248\n"
        "v_bfe_u32 v249, v0, 2, 2\n"
        "v_lshrrev_b32 v1, 5, v0\n"
        "v_lshl_or_b32 v249, v1, 3, v249\n"
        "s_mov_b32 s18, 0x00100001\n"
        "s_mov_b32 s19, 0x00100001\n"
        "v_mov_b32 v1, 1065369472\n"
        "v_cndmask_b32 v228, 0, v1, s[18:19]\n"
        "v_cndmask_b32 v229, 0, v1, s[18:19]\n"
        "v_cndmask_b32 v230, 0, v1, s[18:19]\n"
        "v_cndmask_b32 v231, 0, v1, s[18:19]\n"
        "v_lshrrev_b32 v1, 4, v0\n"
        "v_lshlrev_b32 v2, 2, v1\n"
        "v_xor_b32 v2, v2, v0\n"
        "v_and_b32 v2, 15, v2\n"
        "v_lshlrev_b32 v2, 4, v2\n"
        "v_mul_u32_u24 v1, 3584, v1\n"
        "s_mul_i32 s18, s10, 57344\n"
        "s_lshl_b32 s19, s8, 8\n"
        "s_add_u32 s18, s18, s19\n"
        "s_cmp_ge_u32 s8, 14\n"
        "s_cselect_b32 s18, 1073741824, s18\n"
        "v_add3_u32 v240, v1, v2, s18\n"
        "v_lshl_or_b32 v1, v249, 8, v248\n"
        "v_add_u32 v1, 73728, v1\n"
        "v_bfe_u32 v2, v0, 2, 2\n"
        "s_add_u32 s18, s10, 0\n"
        "s_and_b32 s18, s18, 3\n"
        "v_xor_b32 v3, s18, v2\n"
        "v_lshl_add_u32 v241, v3, 6, v1\n"
        "s_add_u32 s18, s10, 1\n"
        "s_and_b32 s18, s18, 3\n"
        "v_xor_b32 v3, s18, v2\n"
        "v_lshl_add_u32 v242, v3, 6, v1\n"
        "s_add_u32 s18, s10, 2\n"
        "s_and_b32 s18, s18, 3\n"
        "v_xor_b32 v3, s18, v2\n"
        "v_lshl_add_u32 v243, v3, 6, v1\n"
        "s_add_u32 s18, s10, 3\n"
        "s_and_b32 s18, s18, 3\n"
        "v_xor_b32 v3, s18, v2\n"
        "v_lshl_add_u32 v244, v3, 6, v1\n"
        "v_lshlrev_b32 v245, 4, v0\n"
        "v_mov_b32 v246, 159752\n"
        "v_mov_b32 v247, 1\n"
        "v_mov_b32 v250, 1065369472\n"
        "v_mov_b32 v14, 0\n"
        "v_mov_b32 v15, 0\n"
        "s_waitcnt lgkmcnt(0)\n"
        "s_mul_i32 s68, s68, 0x41C64E6D\n"
        "s_add_u32 s68, s68, 0x3039\n"
        "s_mul_i32 s69, s69, 0x0019660D\n"
        "s_add_u32 s69, s69, 0x3C6EF35F\n"
        "s_setprio 0\n"
        "v_lshl_or_b32 v128, v249, 7, v248\n"
        "v_and_b32 v1, 8, v0\n"
        "v_lshl_or_b32 v128, v1, 3, v128\n"
        "s_lshl_b32 s18, s10, 11\n"
        "s_add_u32 s18, s18, 49152\n"
        "v_add_u32 v128, s18, v128\n"
        "v_xor_b32 v129, 64, v128\n"
        "s_lshl_b32 s18, s10, 10\n"
        "s_add_u32 s18, s18, 110592\n"
        "v_lshl_or_b32 v130, v249, 6, v248\n"
        "v_add_u32 v130, s18, v130\n"
        "s_waitcnt vmcnt(8)\n"
        "s_barrier\n"
        "ds_read_b128 v[16:19], v236 offset:0\n"
        "ds_read_b128 v[20:23], v237 offset:0\n"
        "ds_read_b128 v[24:27], v238 offset:0\n"
        "ds_read_b128 v[28:31], v239 offset:0\n"
        "ds_read_b64_tr_b16 v[132:133], v128 offset:0\n"
        "ds_read_b64_tr_b16 v[134:135], v128 offset:512\n"
        "ds_read_b64_tr_b16 v[136:137], v129 offset:0\n"
        "ds_read_b64_tr_b16 v[138:139], v129 offset:512\n"
        "ds_read_b64_tr_b16 v[140:141], v130 offset:0\n"
        "ds_read_b64_tr_b16 v[142:143], v130 offset:256\n"
        "s_waitcnt lgkmcnt(9)\n"
        "v_dot2c_f32_bf16 v14, v16, v250\n"
        "v_dot2c_f32_bf16 v15, v16, v16\n"
        "v_dot2c_f32_bf16 v14, v17, v250\n"
        "v_dot2c_f32_bf16 v15, v17, v17\n"
        "v_dot2c_f32_bf16 v14, v18, v250\n"
        "v_dot2c_f32_bf16 v15, v18, v18\n"
        "v_dot2c_f32_bf16 v14, v19, v250\n"
        "v_dot2c_f32_bf16 v15, v19, v19\n"
        "s_waitcnt lgkmcnt(8)\n"
        "v_dot2c_f32_bf16 v14, v20, v250\n"
        "v_dot2c_f32_bf16 v15, v20, v20\n"
        "v_dot2c_f32_bf16 v14, v21, v250\n"
        "v_dot2c_f32_bf16 v15, v21, v21\n"
        "v_dot2c_f32_bf16 v14, v22, v250\n"
        "v_dot2c_f32_bf16 v15, v22, v22\n"
        "v_dot2c_f32_bf16 v14, v23, v250\n"
        "v_dot2c_f32_bf16 v15, v23, v23\n"
        "s_waitcnt lgkmcnt(7)\n"
        "v_dot2c_f32_bf16 v14, v24, v250\n"
        "v_dot2c_f32_bf16 v15, v24, v24\n"
        "v_dot2c_f32_bf16 v14, v25, v250\n"
        "v_dot2c_f32_bf16 v15, v25, v25\n"
        "v_dot2c_f32_bf16 v14, v26, v250\n"
        "v_dot2c_f32_bf16 v15, v26, v26\n"
        "v_dot2c_f32_bf16 v14, v27, v250\n"
        "v_dot2c_f32_bf16 v15, v27, v27\n"
        "s_waitcnt lgkmcnt(6)\n"
        "v_dot2c_f32_bf16 v14, v28, v250\n"
        "v_dot2c_f32_bf16 v15, v28, v28\n"
        "v_dot2c_f32_bf16 v14, v29, v250\n"
        "v_dot2c_f32_bf16 v15, v29, v29\n"
        "v_dot2c_f32_bf16 v14, v30, v250\n"
        "v_dot2c_f32_bf16 v15, v30, v30\n"
        "v_dot2c_f32_bf16 v14, v31, v250\n"
        "v_dot2c_f32_bf16 v15, v31, v31\n"
        "s_waitcnt lgkmcnt(0)\n"
        "v_mfma_f32_16x16x32_bf16 v[144:147], v[228:231], v[132:135], 0\n"
        "v_mfma_f32_16x16x32_bf16 v[148:151], v[228:231], v[136:139], 0\n"
        "v_mfma_f32_16x16x32_bf16 v[152:155], v[228:231], v[140:143], 0\n"
        "s_waitcnt vmcnt(4)\n"
        "s_barrier\n"
        "ds_read_b128 v[32:35], v236 offset:16384\n"
        "ds_read_b128 v[36:39], v237 offset:16384\n"
        "ds_read_b128 v[40:43], v238 offset:16384\n"
        "ds_read_b128 v[44:47], v239 offset:16384\n"
        "ds_read_b64_tr_b16 v[132:133], v128 offset:8192\n"
        "ds_read_b64_tr_b16 v[134:135], v128 offset:8704\n"
        "ds_read_b64_tr_b16 v[136:137], v129 offset:8192\n"
        "ds_read_b64_tr_b16 v[138:139], v129 offset:8704\n"
        "ds_read_b64_tr_b16 v[140:141], v130 offset:4096\n"
        "ds_read_b64_tr_b16 v[142:143], v130 offset:4352\n"
        "s_add_u32 m0, s70, 0\n"
        "s_lshl_b32 s71, s92, 7\n"
        "buffer_load_dwordx4 v232, s[32:35], s71 offen  lds\n"
        "s_add_u32 m0, s70, 1024\n"
        "s_nop 0\n"
        "buffer_load_dwordx4 v233, s[32:35], s71 offen  lds\n"
        "s_add_u32 m0, s70, 2048\n"
        "s_nop 0\n"
        "buffer_load_dwordx4 v234, s[32:35], s71 offen  lds\n"
        "s_add_u32 m0, s70, 3072\n"
        "s_nop 0\n"
        "buffer_load_dwordx4 v235, s[32:35], s71 offen  lds\n"
        "s_add_u32 m0, s70, 73728\n"
        "s_mul_i32 s83, s11, 229376\n"
        "buffer_load_dwordx4 v240, s[44:47], s83 offen  lds\n"
        "s_waitcnt lgkmcnt(9)\n"
        "v_dot2c_f32_bf16 v14, v32, v250\n"
        "v_dot2c_f32_bf16 v15, v32, v32\n"
        "v_dot2c_f32_bf16 v14, v33, v250\n"
        "v_dot2c_f32_bf16 v15, v33, v33\n"
        "v_dot2c_f32_bf16 v14, v34, v250\n"
        "v_dot2c_f32_bf16 v15, v34, v34\n"
        "v_dot2c_f32_bf16 v14, v35, v250\n"
        "v_dot2c_f32_bf16 v15, v35, v35\n"
        "s_waitcnt lgkmcnt(8)\n"
        "v_dot2c_f32_bf16 v14, v36, v250\n"
        "v_dot2c_f32_bf16 v15, v36, v36\n"
        "v_dot2c_f32_bf16 v14, v37, v250\n"
        "v_dot2c_f32_bf16 v15, v37, v37\n"
        "v_dot2c_f32_bf16 v14, v38, v250\n"
        "v_dot2c_f32_bf16 v15, v38, v38\n"
        "v_dot2c_f32_bf16 v14, v39, v250\n"
        "v_dot2c_f32_bf16 v15, v39, v39\n"
        "s_waitcnt lgkmcnt(7)\n"
        "v_dot2c_f32_bf16 v14, v40, v250\n"
        "v_dot2c_f32_bf16 v15, v40, v40\n"
        "v_dot2c_f32_bf16 v14, v41, v250\n"
        "v_dot2c_f32_bf16 v15, v41, v41\n"
        "v_dot2c_f32_bf16 v14, v42, v250\n"
        "v_dot2c_f32_bf16 v15, v42, v42\n"
        "v_dot2c_f32_bf16 v14, v43, v250\n"
        "v_dot2c_f32_bf16 v15, v43, v43\n"
        "s_waitcnt lgkmcnt(6)\n"
        "v_dot2c_f32_bf16 v14, v44, v250\n"
        "v_dot2c_f32_bf16 v15, v44, v44\n"
        "v_dot2c_f32_bf16 v14, v45, v250\n"
        "v_dot2c_f32_bf16 v15, v45, v45\n"
        "v_dot2c_f32_bf16 v14, v46, v250\n"
        "v_dot2c_f32_bf16 v15, v46, v46\n"
        "v_dot2c_f32_bf16 v14, v47, v250\n"
        "v_dot2c_f32_bf16 v15, v47, v47\n"
        "s_waitcnt lgkmcnt(0)\n"
        "v_mfma_f32_16x16x32_bf16 v[144:147], v[228:231], v[132:135], v[144:147]\n"
        "v_mfma_f32_16x16x32_bf16 v[148:151], v[228:231], v[136:139], v[148:151]\n"
        "v_mfma_f32_16x16x32_bf16 v[152:155], v[228:231], v[140:143], v[152:155]\n"
        "s_waitcnt vmcnt(5)\n"
        "s_barrier\n"
        "ds_read_b128 v[48:51], v236 offset:32768\n"
        "ds_read_b128 v[52:55], v237 offset:32768\n"
        "ds_read_b128 v[56:59], v238 offset:32768\n"
        "ds_read_b128 v[60:63], v239 offset:32768\n"
        "ds_read_b64_tr_b16 v[132:133], v128 offset:16384\n"
        "ds_read_b64_tr_b16 v[134:135], v128 offset:16896\n"
        "ds_read_b64_tr_b16 v[136:137], v129 offset:16384\n"
        "ds_read_b64_tr_b16 v[138:139], v129 offset:16896\n"
        "ds_read_b64_tr_b16 v[140:141], v130 offset:8192\n"
        "ds_read_b64_tr_b16 v[142:143], v130 offset:8448\n"
        "s_add_u32 m0, s70, 16384\n"
        "s_lshl_b32 s71, s93, 7\n"
        "buffer_load_dwordx4 v232, s[32:35], s71 offen  lds\n"
        "s_add_u32 m0, s70, 17408\n"
        "s_nop 0\n"
        "buffer_load_dwordx4 v233, s[32:35], s71 offen  lds\n"
        "s_add_u32 m0, s70, 18432\n"
        "s_nop 0\n"
        "buffer_load_dwordx4 v234, s[32:35], s71 offen  lds\n"
        "s_add_u32 m0, s70, 19456\n"
        "s_nop 0\n"
        "buffer_load_dwordx4 v235, s[32:35], s71 offen  lds\n"
        "s_add_u32 m0, s70, 74752\n"
        "s_add_u32 s83, s83, 14336\n"
        "buffer_load_dwordx4 v240, s[44:47], s83 offen  lds\n"
        "s_waitcnt lgkmcnt(9)\n"
        "v_dot2c_f32_bf16 v14, v48, v250\n"
        "v_dot2c_f32_bf16 v15, v48, v48\n"
        "v_dot2c_f32_bf16 v14, v49, v250\n"
        "v_dot2c_f32_bf16 v15, v49, v49\n"
        "v_dot2c_f32_bf16 v14, v50, v250\n"
        "v_dot2c_f32_bf16 v15, v50, v50\n"
        "v_dot2c_f32_bf16 v14, v51, v250\n"
        "v_dot2c_f32_bf16 v15, v51, v51\n"
        "s_waitcnt lgkmcnt(8)\n"
        "v_dot2c_f32_bf16 v14, v52, v250\n"
        "v_dot2c_f32_bf16 v15, v52, v52\n"
        "v_dot2c_f32_bf16 v14, v53, v250\n"
        "v_dot2c_f32_bf16 v15, v53, v53\n"
        "v_dot2c_f32_bf16 v14, v54, v250\n"
        "v_dot2c_f32_bf16 v15, v54, v54\n"
        "v_dot2c_f32_bf16 v14, v55, v250\n"
        "v_dot2c_f32_bf16 v15, v55, v55\n"
        "s_waitcnt lgkmcnt(7)\n"
        "v_dot2c_f32_bf16 v14, v56, v250\n"
        "v_dot2c_f32_bf16 v15, v56, v56\n"
        "v_dot2c_f32_bf16 v14, v57, v250\n"
        "v_dot2c_f32_bf16 v15, v57, v57\n"
        "v_dot2c_f32_bf16 v14, v58, v250\n"
        "v_dot2c_f32_bf16 v15, v58, v58\n"
        "v_dot2c_f32_bf16 v14, v59, v250\n"
        "v_dot2c_f32_bf16 v15, v59, v59\n"
        "s_waitcnt lgkmcnt(6)\n"
        "v_dot2c_f32_bf16 v14, v60, v250\n"
        "v_dot2c_f32_bf16 v15, v60, v60\n"
        "v_dot2c_f32_bf16 v14, v61, v250\n"
        "v_dot2c_f32_bf16 v15, v61, v61\n"
        "v_dot2c_f32_bf16 v14, v62, v250\n"
        "v_dot2c_f32_bf16 v15, v62, v62\n"
        "v_dot2c_f32_bf16 v14, v63, v250\n"
        "v_dot2c_f32_bf16 v15, v63, v63\n"
        "s_waitcnt lgkmcnt(0)\n"
        "v_mfma_f32_16x16x32_bf16 v[144:147], v[228:231], v[132:135], v[144:147]\n"
        "v_mfma_f32_16x16x32_bf16 v[148:151], v[228:231], v[136:139], v[148:151]\n"
        "v_mfma_f32_16x16x32_bf16 v[152:155], v[228:231], v[140:143], v[152:155]\n"
        "s_waitcnt vmcnt(6)\n"
        "s_barrier\n"
        "ds_read_b128 v[64:67], v236 offset:0\n"
        "ds_read_b128 v[68:71], v237 offset:0\n"
        "ds_read_b128 v[72:75], v238 offset:0\n"
        "ds_read_b128 v[76:79], v239 offset:0\n"
        "ds_read_b64_tr_b16 v[132:133], v128 offset:0\n"
        "ds_read_b64_tr_b16 v[134:135], v128 offset:512\n"
        "ds_read_b64_tr_b16 v[136:137], v129 offset:0\n"
        "ds_read_b64_tr_b16 v[138:139], v129 offset:512\n"
        "ds_read_b64_tr_b16 v[140:141], v130 offset:12288\n"
        "ds_read_b64_tr_b16 v[142:143], v130 offset:12544\n"
        "s_add_u32 m0, s70, 32768\n"
        "s_lshl_b32 s71, s94, 7\n"
        "buffer_load_dwordx4 v232, s[32:35], s71 offen  lds\n"
        "s_add_u32 m0, s70, 33792\n"
        "s_nop 0\n"
        "buffer_load_dwordx4 v233, s[32:35], s71 offen  lds\n"
        "s_add_u32 m0, s70, 34816\n"
        "s_nop 0\n"
        "buffer_load_dwordx4 v234, s[32:35], s71 offen  lds\n"
        "s_add_u32 m0, s70, 35840\n"
        "s_nop 0\n"
        "buffer_load_dwordx4 v235, s[32:35], s71 offen  lds\n"
        "s_add_u32 m0, s70, 75776\n"
        "s_add_u32 s83, s83, 14336\n"
        "buffer_load_dwordx4 v240, s[44:47], s83 offen  lds\n"
        "s_waitcnt lgkmcnt(9)\n"
        "v_dot2c_f32_bf16 v14, v64, v250\n"
        "v_dot2c_f32_bf16 v15, v64, v64\n"
        "v_dot2c_f32_bf16 v14, v65, v250\n"
        "v_dot2c_f32_bf16 v15, v65, v65\n"
        "v_dot2c_f32_bf16 v14, v66, v250\n"
        "v_dot2c_f32_bf16 v15, v66, v66\n"
        "v_dot2c_f32_bf16 v14, v67, v250\n"
        "v_dot2c_f32_bf16 v15, v67, v67\n"
        "s_waitcnt lgkmcnt(8)\n"
        "v_dot2c_f32_bf16 v14, v68, v250\n"
        "v_dot2c_f32_bf16 v15, v68, v68\n"
        "v_dot2c_f32_bf16 v14, v69, v250\n"
        "v_dot2c_f32_bf16 v15, v69, v69\n"
        "v_dot2c_f32_bf16 v14, v70, v250\n"
        "v_dot2c_f32_bf16 v15, v70, v70\n"
        "v_dot2c_f32_bf16 v14, v71, v250\n"
        "v_dot2c_f32_bf16 v15, v71, v71\n"
        "s_waitcnt lgkmcnt(7)\n"
        "v_dot2c_f32_bf16 v14, v72, v250\n"
        "v_dot2c_f32_bf16 v15, v72, v72\n"
        "v_dot2c_f32_bf16 v14, v73, v250\n"
        "v_dot2c_f32_bf16 v15, v73, v73\n"
        "v_dot2c_f32_bf16 v14, v74, v250\n"
        "v_dot2c_f32_bf16 v15, v74, v74\n"
        "v_dot2c_f32_bf16 v14, v75, v250\n"
        "v_dot2c_f32_bf16 v15, v75, v75\n"
        "s_waitcnt lgkmcnt(6)\n"
        "v_dot2c_f32_bf16 v14, v76, v250\n"
        "v_dot2c_f32_bf16 v15, v76, v76\n"
        "v_dot2c_f32_bf16 v14, v77, v250\n"
        "v_dot2c_f32_bf16 v15, v77, v77\n"
        "v_dot2c_f32_bf16 v14, v78, v250\n"
        "v_dot2c_f32_bf16 v15, v78, v78\n"
        "v_dot2c_f32_bf16 v14, v79, v250\n"
        "v_dot2c_f32_bf16 v15, v79, v79\n"
        "s_waitcnt lgkmcnt(0)\n"
        "v_mfma_f32_16x16x32_bf16 v[144:147], v[228:231], v[132:135], v[144:147]\n"
        "v_mfma_f32_16x16x32_bf16 v[148:151], v[228:231], v[136:139], v[148:151]\n"
        "v_mfma_f32_16x16x32_bf16 v[152:155], v[228:231], v[140:143], v[152:155]\n"
        "s_waitcnt vmcnt(6)\n"
        "s_barrier\n"
        "ds_read_b128 v[80:83], v236 offset:16384\n"
        "ds_read_b128 v[84:87], v237 offset:16384\n"
        "ds_read_b128 v[88:91], v238 offset:16384\n"
        "ds_read_b128 v[92:95], v239 offset:16384\n"
        "ds_read_b64_tr_b16 v[132:133], v128 offset:8192\n"
        "ds_read_b64_tr_b16 v[134:135], v128 offset:8704\n"
        "ds_read_b64_tr_b16 v[136:137], v129 offset:8192\n"
        "ds_read_b64_tr_b16 v[138:139], v129 offset:8704\n"
        "ds_read_b64_tr_b16 v[140:141], v130 offset:16384\n"
        "ds_read_b64_tr_b16 v[142:143], v130 offset:16640\n"
        "s_add_u32 m0, s70, 0\n"
        "s_lshl_b32 s71, s95, 7\n"
        "buffer_load_dwordx4 v232, s[32:35], s71 offen  lds\n"
        "s_add_u32 m0, s70, 1024\n"
        "s_nop 0\n"
        "buffer_load_dwordx4 v233, s[32:35], s71 offen  lds\n"
        "s_add_u32 m0, s70, 2048\n"
        "s_nop 0\n"
        "buffer_load_dwordx4 v234, s[32:35], s71 offen  lds\n"
        "s_add_u32 m0, s70, 3072\n"
        "s_nop 0\n"
        "buffer_load_dwordx4 v235, s[32:35], s71 offen  lds\n"
        "s_add_u32 m0, s70, 76800\n"
        "s_add_u32 s83, s83, 14336\n"
        "buffer_load_dwordx4 v240, s[44:47], s83 offen  lds\n"
        "s_waitcnt lgkmcnt(9)\n"
        "v_dot2c_f32_bf16 v14, v80, v250\n"
        "v_dot2c_f32_bf16 v15, v80, v80\n"
        "v_dot2c_f32_bf16 v14, v81, v250\n"
        "v_dot2c_f32_bf16 v15, v81, v81\n"
        "v_dot2c_f32_bf16 v14, v82, v250\n"
        "v_dot2c_f32_bf16 v15, v82, v82\n"
        "v_dot2c_f32_bf16 v14, v83, v250\n"
        "v_dot2c_f32_bf16 v15, v83, v83\n"
        "s_waitcnt lgkmcnt(8)\n"
        "v_dot2c_f32_bf16 v14, v84, v250\n"
        "v_dot2c_f32_bf16 v15, v84, v84\n"
        "v_dot2c_f32_bf16 v14, v85, v250\n"
        "v_dot2c_f32_bf16 v15, v85, v85\n"
        "v_dot2c_f32_bf16 v14, v86, v250\n"
        "v_dot2c_f32_bf16 v15, v86, v86\n"
        "v_dot2c_f32_bf16 v14, v87, v250\n"
        "v_dot2c_f32_bf16 v15, v87, v87\n"
        "s_waitcnt lgkmcnt(7)\n"
        "v_dot2c_f32_bf16 v14, v88, v250\n"
        "v_dot2c_f32_bf16 v15, v88, v88\n"
        "v_dot2c_f32_bf16 v14, v89, v250\n"
        "v_dot2c_f32_bf16 v15, v89, v89\n"
        "v_dot2c_f32_bf16 v14, v90, v250\n"
        "v_dot2c_f32_bf16 v15, v90, v90\n"
        "v_dot2c_f32_bf16 v14, v91, v250\n"
        "v_dot2c_f32_bf16 v15, v91, v91\n"
        "s_waitcnt lgkmcnt(6)\n"
        "v_dot2c_f32_bf16 v14, v92, v250\n"
        "v_dot2c_f32_bf16 v15, v92, v92\n"
        "v_dot2c_f32_bf16 v14, v93, v250\n"
        "v_dot2c_f32_bf16 v15, v93, v93\n"
        "v_dot2c_f32_bf16 v14, v94, v250\n"
        "v_dot2c_f32_bf16 v15, v94, v94\n"
        "v_dot2c_f32_bf16 v14, v95, v250\n"
        "v_dot2c_f32_bf16 v15, v95, v95\n"
        "s_waitcnt lgkmcnt(0)\n"
        "v_mfma_f32_16x16x32_bf16 v[144:147], v[228:231], v[132:135], v[144:147]\n"
        "v_mfma_f32_16x16x32_bf16 v[148:151], v[228:231], v[136:139], v[148:151]\n"
        "v_mfma_f32_16x16x32_bf16 v[152:155], v[228:231], v[140:143], v[152:155]\n"
        "s_waitcnt vmcnt(6)\n"
        "s_barrier\n"
        "ds_read_b128 v[96:99], v236 offset:32768\n"
        "ds_read_b128 v[100:103], v237 offset:32768\n"
        "ds_read_b128 v[104:107], v238 offset:32768\n"
        "ds_read_b128 v[108:111], v239 offset:32768\n"
        "ds_read_b64_tr_b16 v[132:133], v128 offset:16384\n"
        "ds_read_b64_tr_b16 v[134:135], v128 offset:16896\n"
        "ds_read_b64_tr_b16 v[136:137], v129 offset:16384\n"
        "ds_read_b64_tr_b16 v[138:139], v129 offset:16896\n"
        "ds_read_b64_tr_b16 v[140:141], v130 offset:20480\n"
        "ds_read_b64_tr_b16 v[142:143], v130 offset:20736\n"
        "s_add_u32 m0, s70, 90112\n"
        "s_mul_i32 s83, s90, 229376\n"
        "buffer_load_dwordx4 v240, s[44:47], s83 offen  lds\n"
        "s_add_u32 m0, s70, 91136\n"
        "s_add_u32 s83, s83, 14336\n"
        "buffer_load_dwordx4 v240, s[44:47], s83 offen  lds\n"
        "s_waitcnt lgkmcnt(9)\n"
        "v_dot2c_f32_bf16 v14, v96, v250\n"
        "v_dot2c_f32_bf16 v15, v96, v96\n"
        "v_dot2c_f32_bf16 v14, v97, v250\n"
        "v_dot2c_f32_bf16 v15, v97, v97\n"
        "v_dot2c_f32_bf16 v14, v98, v250\n"
        "v_dot2c_f32_bf16 v15, v98, v98\n"
        "v_dot2c_f32_bf16 v14, v99, v250\n"
        "v_dot2c_f32_bf16 v15, v99, v99\n"
        "s_waitcnt lgkmcnt(8)\n"
        "v_dot2c_f32_bf16 v14, v100, v250\n"
        "v_dot2c_f32_bf16 v15, v100, v100\n"
        "v_dot2c_f32_bf16 v14, v101, v250\n"
        "v_dot2c_f32_bf16 v15, v101, v101\n"
        "v_dot2c_f32_bf16 v14, v102, v250\n"
        "v_dot2c_f32_bf16 v15, v102, v102\n"
        "v_dot2c_f32_bf16 v14, v103, v250\n"
        "v_dot2c_f32_bf16 v15, v103, v103\n"
        "s_waitcnt lgkmcnt(7)\n"
        "v_dot2c_f32_bf16 v14, v104, v250\n"
        "v_dot2c_f32_bf16 v15, v104, v104\n"
        "v_dot2c_f32_bf16 v14, v105, v250\n"
        "v_dot2c_f32_bf16 v15, v105, v105\n"
        "v_dot2c_f32_bf16 v14, v106, v250\n"
        "v_dot2c_f32_bf16 v15, v106, v106\n"
        "v_dot2c_f32_bf16 v14, v107, v250\n"
        "v_dot2c_f32_bf16 v15, v107, v107\n"
        "s_waitcnt lgkmcnt(6)\n"
        "v_dot2c_f32_bf16 v14, v108, v250\n"
        "v_dot2c_f32_bf16 v15, v108, v108\n"
        "v_dot2c_f32_bf16 v14, v109, v250\n"
        "v_dot2c_f32_bf16 v15, v109, v109\n"
        "v_dot2c_f32_bf16 v14, v110, v250\n"
        "v_dot2c_f32_bf16 v15, v110, v110\n"
        "v_dot2c_f32_bf16 v14, v111, v250\n"
        "v_dot2c_f32_bf16 v15, v111, v111\n"
        "s_waitcnt lgkmcnt(0)\n"
        "v_mfma_f32_16x16x32_bf16 v[144:147], v[228:231], v[132:135], v[144:147]\n"
        "v_mfma_f32_16x16x32_bf16 v[148:151], v[228:231], v[136:139], v[148:151]\n"
        "v_mfma_f32_16x16x32_bf16 v[152:155], v[228:231], v[140:143], v[152:155]\n"
        "s_waitcnt vmcnt(3)\n"
        "s_barrier\n"
        "ds_read_b128 v[112:115], v236 offset:0\n"
        "ds_read_b128 v[116:119], v237 offset:0\n"
        "ds_read_b128 v[120:123], v238 offset:0\n"
        "ds_read_b128 v[124:127], v239 offset:0\n"
        "ds_read_b64_tr_b16 v[132:133], v128 offset:0\n"
        "ds_read_b64_tr_b16 v[134:135], v128 offset:512\n"
        "ds_read_b64_tr_b16 v[136:137], v129 offset:0\n"
        "ds_read_b64_tr_b16 v[138:139], v129 offset:512\n"
        "ds_read_b64_tr_b16 v[140:141], v130 offset:24576\n"
        "ds_read_b64_tr_b16 v[142:143], v130 offset:24832\n"
        "s_add_u32 m0, s70, 92160\n"
        "s_add_u32 s83, s83, 14336\n"
        "buffer_load_dwordx4 v240, s[44:47], s83 offen  lds\n"
        "s_add_u32 m0, s70, 93184\n"
        "s_add_u32 s83, s83, 14336\n"
        "buffer_load_dwordx4 v240, s[44:47], s83 offen  lds\n"
        "s_waitcnt lgkmcnt(9)\n"
        "v_dot2c_f32_bf16 v14, v112, v250\n"
        "v_dot2c_f32_bf16 v15, v112, v112\n"
        "v_dot2c_f32_bf16 v14, v113, v250\n"
        "v_dot2c_f32_bf16 v15, v113, v113\n"
        "v_dot2c_f32_bf16 v14, v114, v250\n"
        "v_dot2c_f32_bf16 v15, v114, v114\n"
        "v_dot2c_f32_bf16 v14, v115, v250\n"
        "v_dot2c_f32_bf16 v15, v115, v115\n"
        "s_waitcnt lgkmcnt(0)\n"
        "v_mfma_f32_16x16x32_bf16 v[144:147], v[228:231], v[132:135], v[144:147]\n"
        "v_mfma_f32_16x16x32_bf16 v[148:151], v[228:231], v[136:139], v[148:151]\n"
        "v_mfma_f32_16x16x32_bf16 v[152:155], v[228:231], v[140:143], v[152:155]\n"
        "v_dot2c_f32_bf16 v14, v116, v250\n"
        "v_dot2c_f32_bf16 v15, v116, v116\n"
        "v_dot2c_f32_bf16 v14, v117, v250\n"
        "v_dot2c_f32_bf16 v15, v117, v117\n"
        "v_dot2c_f32_bf16 v14, v118, v250\n"
        "v_dot2c_f32_bf16 v15, v118, v118\n"
        "v_dot2c_f32_bf16 v14, v119, v250\n"
        "v_dot2c_f32_bf16 v15, v119, v119\n"
        "v_dot2c_f32_bf16 v14, v120, v250\n"
        "v_dot2c_f32_bf16 v15, v120, v120\n"
        "v_dot2c_f32_bf16 v14, v121, v250\n"
        "v_dot2c_f32_bf16 v15, v121, v121\n"
        "v_dot2c_f32_bf16 v14, v122, v250\n"
        "v_dot2c_f32_bf16 v15, v122, v122\n"
        "v_dot2c_f32_bf16 v14, v123, v250\n"
        "v_dot2c_f32_bf16 v15, v123, v123\n"
        "v_dot2c_f32_bf16 v14, v124, v250\n"
        "v_dot2c_f32_bf16 v15, v124, v124\n"
        "v_dot2c_f32_bf16 v14, v125, v250\n"
        "v_dot2c_f32_bf16 v15, v125, v125\n"
        "v_dot2c_f32_bf16 v14, v126, v250\n"
        "v_dot2c_f32_bf16 v15, v126, v126\n"
        "v_dot2c_f32_bf16 v14, v127, v250\n"
        "v_dot2c_f32_bf16 v15, v127, v127\n"
        "s_lshl_b32 s18, s10, 2\n"
        "s_add_u32 s18, s18, 106496\n"
        "v_add_u32 v6, s18, v245\n"
        "ds_write_b32 v6, v144\n"
        "ds_write_b32 v6, v148 offset:1024\n"
        "ds_write_b32 v6, v152 offset:2048\n"
        "v_mov_b32 v1, v14\n"
        "v_mov_b32 v2, v15\n"
        "v_and_b32 v5, 31, v0\n"
        "s_nop 0\n"
        "v_permlane32_swap_b32 v1, v14\n"
        "v_permlane32_swap_b32 v2, v15\n"
        "v_add_f32 v1, v1, v14\n"
        "v_add_f32 v2, v2, v15\n"
        "v_mul_f32 v1, 0xbb124925, v1\n"
        "v_mul_f32 v2, 0x3b124925, v2\n"
        "v_fma_f32 v2, -v1, v1, v2\n"
        "v_max_f32 v2, 0, v2\n"
        "v_add_f32 v2, 0x3727c5ac, v2\n"
        "v_rsq_f32 v12, v2\n"
        "v_cvt_pk_bf16_f32 v8, v1, v1\n"
        "v_and_b32 v3, 0xffff0000, v8\n"
        "v_sub_f32 v3, v1, v3\n"
        "v_mul_f32 v2, v2, v12\n"
        "v_cvt_pk_bf16_f32 v9, v3, v2\n"
        "v_and_b32 v3, 0xffff0000, v9\n"
        "v_sub_f32 v3, v2, v3\n"
        "v_cvt_pk_bf16_f32 v10, v3, 0\n"
        "v_mov_b32 v11, v12\n"
        "s_lshl_b32 s18, s10, 9\n"
        "s_add_u32 s18, s18, 160768\n"
        "v_lshlrev_b32 v6, 4, v5\n"
        "v_add_u32 v6, s18, v6\n"
        "ds_write_b128 v6, v[8:11]\n"
        "s_lshl_b32 s18, s10, 7\n"
        "s_add_u32 s18, s18, 160000\n"
        "v_lshlrev_b32 v6, 2, v5\n"
        "v_add_u32 v6, s18, v6\n"
        "ds_write_b32 v6, v12\n"
        "s_cmp_lt_u32 s8, 14\n"
        "s_cbranch_scc1 .LG_act\n"
        "s_waitcnt vmcnt(0)\n"
        ".LG_act:\n"
        "s_waitcnt lgkmcnt(0)\n"
        "s_barrier\n"
        "v_mov_b32 v11, 0\n"
        "s_cmp_lt_u32 s8, 14\n"
        "s_cbranch_scc1 .LG_ffn1\n"
        "s_mul_i32 s73, s12, 37\n"
        "s_lshr_b32 s73, s73, 8\n"
        "s_mul_i32 s18, s73, 7\n"
        "s_sub_u32 s74, s12, s18\n"
        "s_lshl_b32 s75, s13, 2\n"
        "s_add_u32 s75, s75, s73\n"
        "s_lshl_b32 s76, s10, 11\n"
        "s_mul_i32 s79, s10, 28\n"
        "s_add_u32 s78, s79, 28\n"
        "s_lshl_b32 s19, s74, 2\n"
        "s_add_u32 s18, s18, s19\n"
        "s_sub_u32 s19, s18, 28\n"
        "s_cselect_b32 s18, s18, s19\n"
        "s_add_u32 s77, s79, s18\n"
        "s_mov_b32 s96, s77\n"
        "v_lshrrev_b32 v2, 2, v0\n"
        "v_and_b32 v2, 4, v2\n"
        "v_xor_b32 v2, v2, v0\n"
        "v_and_b32 v2, 7, v2\n"
        "v_lshlrev_b32 v2, 4, v2\n"
        "v_lshrrev_b32 v232, 3, v0\n"
        "v_mul_u32_u24 v232, 896, v232\n"
        "v_add_u32 v232, v232, v2\n"
        "s_lshl_b32 s18, s74, 7\n"
        "s_cmp_ge_u32 s12, 28\n"
        "s_cselect_b32 s18, 1073741824, s18\n"
        "v_add_u32 v232, s18, v232\n"
        "v_add_u32 v233, 7168, v232\n"
        "s_and_b32 s18, s10, 1\n"
        "s_lshr_b32 s19, s10, 1\n"
        "s_lshl_b32 s20, s75, 1\n"
        "s_add_u32 s21, s20, s18\n"
        "s_lshl_b32 s97, s74, 7\n"
        "s_lshl_b32 s24, s19, 6\n"
        "s_add_u32 s97, s97, s24\n"
        "v_lshrrev_b32 v3, 5, v0\n"
        "v_lshlrev_b32 v3, 4, v3\n"
        "v_add_u32 v3, s97, v3\n"
        "v_and_b32 v2, 31, v0\n"
        "v_mul_u32_u24 v2, 896, v2\n"
        "s_mul_i32 s24, s21, 28672\n"
        "v_add3_u32 v238, v3, v2, s24\n"
        "s_mul_i32 s24, s21, 114688\n"
        "s_add_u32 s24, s24, 6291456\n"
        "v_add_u32 v234, s24, v245\n"
        "s_xor_b32 s21, s21, 1\n"
        "s_mul_i32 s24, s21, 114688\n"
        "s_add_u32 s24, s24, 6291456\n"
        "v_add_u32 v235, s24, v245\n"
        "v_lshl_or_b32 v236, v249, 7, v248\n"
        "v_and_b32 v2, 8, v0\n"
        "v_lshl_or_b32 v236, v2, 3, v236\n"
        "s_lshl_b32 s24, s19, 6\n"
        "v_xor_b32 v236, s24, v236\n"
        "v_xor_b32 v237, 64, v236\n"
        "s_mul_i32 s18, s10, 7\n"
        "s_lshr_b32 s18, s18, 1\n"
        "v_and_b32 v2, 3, v0\n"
        "v_add_u32 v2, s18, v2\n"
        "s_lshr_b32 s18, s75, 1\n"
        "v_lshl_add_u32 v2, v2, 4, s18\n"
        "v_lshlrev_b32 v2, 7, v2\n"
        "v_add_u32 v239, 36864, v2\n"
        "s_setprio 1\n"
        "s_branch .LG_ffn2\n"
        ".LG_ffn1:\n"
        "v_and_b32 v1, 31, v0\n"
        "v_lshrrev_b32 v2, 1, v1\n"
        "v_xor_b32 v2, v2, v1\n"
        "v_and_b32 v2, 4, v2\n"
        "v_lshl_or_b32 v2, v2, 1, v2\n"
        "v_xor_b32 v1, v1, v2\n"
        "v_lshlrev_b32 v1, 1, v1\n"
        "v_mov_b32 v2, 1073741824\n"
        "v_cndmask_b32 v1, v1, v2, s[16:17]\n"
        "s_add_u32 s18, s10, 0\n"
        "s_and_b32 s18, s18, 3\n"
        "s_lshl_b32 s18, s18, 6\n"
        "s_lshl_b32 s19, s8, 8\n"
        "s_add_u32 s18, s18, s19\n"
        "buffer_load_ushort v252, v1, s[48:51], s18 offen\n"
        "s_add_u32 s18, s10, 1\n"
        "s_and_b32 s18, s18, 3\n"
        "s_lshl_b32 s18, s18, 6\n"
        "s_lshl_b32 s19, s8, 8\n"
        "s_add_u32 s18, s18, s19\n"
        "buffer_load_ushort v253, v1, s[48:51], s18 offen\n"
        "s_add_u32 s18, s10, 2\n"
        "s_and_b32 s18, s18, 3\n"
        "s_lshl_b32 s18, s18, 6\n"
        "s_lshl_b32 s19, s8, 8\n"
        "s_add_u32 s18, s18, s19\n"
        "buffer_load_ushort v254, v1, s[48:51], s18 offen\n"
        "s_add_u32 s18, s10, 3\n"
        "s_and_b32 s18, s18, 3\n"
        "s_lshl_b32 s18, s18, 6\n"
        "s_lshl_b32 s19, s8, 8\n"
        "s_add_u32 s18, s18, s19\n"
        "buffer_load_ushort v255, v1, s[48:51], s18 offen\n"
        "s_lshl_b32 s18, s8, 4\n"
        "s_add_u32 s18, s18, s9\n"
        "s_lshl_b32 s18, s18, 2\n"
        "s_add_u32 s18, s18, s10\n"
        "s_mul_i32 s19, s18, 1792\n"
        "s_mul_i32 s18, s18, 448\n"
        "v_lshlrev_b32 v1, 7, v0\n"
        "s_mov_b64 exec, 0x3fff\n"
        "buffer_load_dword v4, v1, s[52:55], s19 offen\n"
        "s_mov_b64 exec, 15\n"
        "buffer_load_dword v5, v1, s[40:43], s18 offen\n"
        "s_mov_b64 exec, -1\n"
        "s_waitcnt vmcnt(10)\n"
        "s_add_u32 s14, s14, 4\n"
        "s_mov_b64 exec, 1\n"
        "ds_add_u32 v246, v247\n"
        "s_mov_b64 exec, -1\n"
        "s_mul_i32 s73, s12, 37\n"
        "s_lshr_b32 s73, s73, 8\n"
        "s_mul_i32 s18, s73, 7\n"
        "s_sub_u32 s74, s12, s18\n"
        "s_lshl_b32 s75, s13, 2\n"
        "s_add_u32 s75, s75, s73\n"
        "s_lshl_b32 s76, s10, 11\n"
        "s_mul_i32 s79, s10, 28\n"
        "s_add_u32 s78, s79, 28\n"
        "s_lshl_b32 s19, s74, 2\n"
        "s_add_u32 s18, s18, s19\n"
        "s_sub_u32 s19, s18, 28\n"
        "s_cselect_b32 s18, s18, s19\n"
        "s_add_u32 s77, s79, s18\n"
        "s_mov_b32 s96, s77\n"
        "v_lshrrev_b32 v2, 2, v0\n"
        "v_and_b32 v2, 4, v2\n"
        "v_xor_b32 v2, v2, v0\n"
        "v_and_b32 v2, 7, v2\n"
        "v_lshlrev_b32 v2, 4, v2\n"
        "v_lshrrev_b32 v232, 3, v0\n"
        "v_mul_u32_u24 v232, 896, v232\n"
        "v_add_u32 v232, v232, v2\n"
        "s_lshl_b32 s18, s74, 7\n"
        "s_cmp_ge_u32 s12, 28\n"
        "s_cselect_b32 s18, 1073741824, s18\n"
        "v_add_u32 v232, s18, v232\n"
        "v_add_u32 v233, 7168, v232\n"
        "s_and_b32 s18, s10, 1\n"
        "s_lshr_b32 s19, s10, 1\n"
        "s_lshl_b32 s20, s75, 1\n"
        "s_add_u32 s21, s20, s18\n"
        "s_lshl_b32 s97, s74, 7\n"
        "s_lshl_b32 s24, s19, 6\n"
        "s_add_u32 s97, s97, s24\n"
        "v_lshrrev_b32 v3, 5, v0\n"
        "v_lshlrev_b32 v3, 4, v3\n"
        "v_add_u32 v3, s97, v3\n"
        "v_and_b32 v2, 31, v0\n"
        "v_mul_u32_u24 v2, 896, v2\n"
        "s_mul_i32 s24, s21, 28672\n"
        "v_add3_u32 v238, v3, v2, s24\n"
        "s_mul_i32 s24, s21, 114688\n"
        "s_add_u32 s24, s24, 6291456\n"
        "v_add_u32 v234, s24, v245\n"
        "s_xor_b32 s21, s21, 1\n"
        "s_mul_i32 s24, s21, 114688\n"
        "s_add_u32 s24, s24, 6291456\n"
        "v_add_u32 v235, s24, v245\n"
        "v_lshl_or_b32 v236, v249, 7, v248\n"
        "v_and_b32 v2, 8, v0\n"
        "v_lshl_or_b32 v236, v2, 3, v236\n"
        "s_lshl_b32 s24, s19, 6\n"
        "v_xor_b32 v236, s24, v236\n"
        "v_xor_b32 v237, 64, v236\n"
        "s_mul_i32 s18, s10, 7\n"
        "s_lshr_b32 s18, s18, 1\n"
        "v_and_b32 v2, 3, v0\n"
        "v_add_u32 v2, s18, v2\n"
        "s_lshr_b32 s18, s75, 1\n"
        "v_lshl_add_u32 v2, v2, 4, s18\n"
        "v_lshlrev_b32 v2, 7, v2\n"
        "v_add_u32 v239, 36864, v2\n"
        "s_mov_b32 s26, 0\n"
        ".Lspin_23:\n"
        "ds_read_b32 v1, v246\n"
        "s_waitcnt lgkmcnt(0)\n"
        ".Lsmid_25:\n"
        "v_readfirstlane_b32 s27, v1\n"
        "s_add_u32 s26, s26, 1\n"
        "s_and_b32 s25, s27, 0xffff\n"
        "s_cmp_ge_u32 s25, s14\n"
        "s_cbranch_scc1 .Lsdone_24\n"
        "s_cmp_lt_u32 s26, 400000\n"
        "s_cbranch_scc1 .Lspin_23\n"
        ".Lsdone_24:\n"
        "s_setprio 0\n"
        "ds_read_b64_tr_b16 v[192:193], v241 offset:0\n"
        "ds_read_b64_tr_b16 v[194:195], v241 offset:1024\n"
        "ds_read_b64_tr_b16 v[196:197], v242 offset:0\n"
        "ds_read_b64_tr_b16 v[198:199], v242 offset:1024\n"
        "ds_read_b64_tr_b16 v[200:201], v243 offset:0\n"
        "ds_read_b64_tr_b16 v[202:203], v243 offset:1024\n"
        "ds_read_b64_tr_b16 v[204:205], v244 offset:0\n"
        "ds_read_b64_tr_b16 v[206:207], v244 offset:1024\n"
        "s_waitcnt lgkmcnt(6)\n"
        "v_mfma_f32_32x32x16_bf16 v[128:143], v[192:195], v[16:19], 0\n"
        "ds_read_b64_tr_b16 v[208:209], v241 offset:4096\n"
        "ds_read_b64_tr_b16 v[210:211], v241 offset:5120\n"
        "s_add_u32 m0, s70, 0\n"
        "s_waitcnt lgkmcnt(6)\n"
        "v_mfma_f32_32x32x16_bf16 v[144:159], v[196:199], v[16:19], 0\n"
        "ds_read_b64_tr_b16 v[212:213], v242 offset:4096\n"
        "ds_read_b64_tr_b16 v[214:215], v242 offset:5120\n"
        "s_mul_i32 s71, s91, 229376\n"
        "s_waitcnt lgkmcnt(6)\n"
        "v_mfma_f32_32x32x16_bf16 v[160:175], v[200:203], v[16:19], 0\n"
        "ds_read_b64_tr_b16 v[216:217], v243 offset:4096\n"
        "ds_read_b64_tr_b16 v[218:219], v243 offset:5120\n"
        "buffer_load_dwordx4 v240, s[44:47], s71 offen  lds\n"
        "s_waitcnt lgkmcnt(6)\n"
        "v_mfma_f32_32x32x16_bf16 v[176:191], v[204:207], v[16:19], 0\n"
        "ds_read_b64_tr_b16 v[220:221], v244 offset:4096\n"
        "ds_read_b64_tr_b16 v[222:223], v244 offset:5120\n"
        "s_add_u32 m0, s70, 1024\n"
        "v_mfma_f32_16x16x32_bf16 v[224:227], v[228:231], v[192:195], 0\n"
        "s_waitcnt lgkmcnt(6)\n"
        "v_mfma_f32_32x32x16_bf16 v[128:143], v[208:211], v[20:23], v[128:143]\n"
        "ds_read_b64_tr_b16 v[192:193], v241 offset:8192\n"
        "ds_read_b64_tr_b16 v[194:195], v241 offset:9216\n"
        "s_add_u32 s71, s71, 14336\n"
        "s_waitcnt lgkmcnt(6)\n"
        "v_mfma_f32_32x32x16_bf16 v[144:159], v[212:215], v[20:23], v[144:159]\n"
        "ds_read_b64_tr_b16 v[196:197], v242 offset:8192\n"
        "ds_read_b64_tr_b16 v[198:199], v242 offset:9216\n"
        "s_waitcnt vmcnt(7)\n"
        "s_add_u32 s14, s14, 4\n"
        "s_mov_b64 exec, 1\n"
        "ds_add_u32 v246, v247\n"
        "s_mov_b64 exec, -1\n"
        "buffer_load_dwordx4 v240, s[44:47], s71 offen  lds\n"
        "s_waitcnt lgkmcnt(7)\n"
        "v_mfma_f32_32x32x16_bf16 v[160:175], v[216:219], v[20:23], v[160:175]\n"
        "ds_read_b64_tr_b16 v[200:201], v243 offset:8192\n"
        "ds_read_b64_tr_b16 v[202:203], v243 offset:9216\n"
        "s_add_u32 m0, s70, 2048\n"
        "s_waitcnt lgkmcnt(7)\n"
        "v_mfma_f32_32x32x16_bf16 v[176:191], v[220:223], v[20:23], v[176:191]\n"
        "ds_read_b64_tr_b16 v[204:205], v244 offset:8192\n"
        "ds_read_b64_tr_b16 v[206:207], v244 offset:9216\n"
        "s_add_u32 s71, s71, 14336\n"
        "v_mfma_f32_16x16x32_bf16 v[224:227], v[228:231], v[208:211], v[224:227]\n"
        "s_waitcnt lgkmcnt(7)\n"
        "v_mfma_f32_32x32x16_bf16 v[128:143], v[192:195], v[24:27], v[128:143]\n"
        "ds_read_b64_tr_b16 v[208:209], v241 offset:12288\n"
        "ds_read_b64_tr_b16 v[210:211], v241 offset:13312\n"
        "buffer_load_dwordx4 v240, s[44:47], s71 offen  lds\n"
        "s_waitcnt lgkmcnt(7)\n"
        "v_mfma_f32_32x32x16_bf16 v[144:159], v[196:199], v[24:27], v[144:159]\n"
        "ds_read_b64_tr_b16 v[212:213], v242 offset:12288\n"
        "ds_read_b64_tr_b16 v[214:215], v242 offset:13312\n"
        "s_add_u32 m0, s70, 3072\n"
        "s_waitcnt lgkmcnt(6)\n"
        "v_mfma_f32_32x32x16_bf16 v[160:175], v[200:203], v[24:27], v[160:175]\n"
        "ds_read_b64_tr_b16 v[216:217], v243 offset:12288\n"
        "ds_read_b64_tr_b16 v[218:219], v243 offset:13312\n"
        "s_add_u32 s71, s71, 14336\n"
        "s_waitcnt lgkmcnt(6)\n"
        "v_mfma_f32_32x32x16_bf16 v[176:191], v[204:207], v[24:27], v[176:191]\n"
        "ds_read_b64_tr_b16 v[220:221], v244 offset:12288\n"
        "ds_read_b64_tr_b16 v[222:223], v244 offset:13312\n"
        "buffer_load_dwordx4 v240, s[44:47], s71 offen  lds\n"
        "v_mfma_f32_16x16x32_bf16 v[224:227], v[228:231], v[192:195], v[224:227]\n"
        "ds_read_b32 v1, v246\n"
        "s_waitcnt lgkmcnt(7)\n"
        "v_mfma_f32_32x32x16_bf16 v[128:143], v[208:211], v[28:31], v[128:143]\n"
        "s_mov_b32 s26, 0\n"
        "s_waitcnt lgkmcnt(0)\n"
        "s_branch .Lsmid_28\n"
        ".Lspin_26:\n"
        "ds_read_b32 v1, v246\n"
        "s_waitcnt lgkmcnt(0)\n"
        ".Lsmid_28:\n"
        "s_setprio 1\n"
        "v_readfirstlane_b32 s27, v1\n"
        "s_setprio 0\n"
        "s_add_u32 s26, s26, 1\n"
        "s_and_b32 s25, s27, 0xffff\n"
        "s_cmp_ge_u32 s25, s14\n"
        "s_cbranch_scc1 .Lsdone_27\n"
        "s_cmp_lt_u32 s26, 400000\n"
        "s_cbranch_scc1 .Lspin_26\n"
        ".Lsdone_27:\n"
        "ds_read_b64_tr_b16 v[192:193], v241 offset:16384\n"
        "ds_read_b64_tr_b16 v[194:195], v241 offset:17408\n"
        "s_add_u32 m0, s70, 16384\n"
        "v_mfma_f32_32x32x16_bf16 v[144:159], v[212:215], v[28:31], v[144:159]\n"
        "ds_read_b64_tr_b16 v[196:197], v242 offset:16384\n"
        "ds_read_b64_tr_b16 v[198:199], v242 offset:17408\n"
        "s_mul_i32 s71, s92, 229376\n"
        "v_mfma_f32_32x32x16_bf16 v[160:175], v[216:219], v[28:31], v[160:175]\n"
        "ds_read_b64_tr_b16 v[200:201], v243 offset:16384\n"
        "ds_read_b64_tr_b16 v[202:203], v243 offset:17408\n"
        "buffer_load_dwordx4 v240, s[44:47], s71 offen  lds\n"
        "v_mfma_f32_32x32x16_bf16 v[176:191], v[220:223], v[28:31], v[176:191]\n"
        "ds_read_b64_tr_b16 v[204:205], v244 offset:16384\n"
        "ds_read_b64_tr_b16 v[206:207], v244 offset:17408\n"
        "s_add_u32 m0, s70, 17408\n"
        "v_mfma_f32_16x16x32_bf16 v[224:227], v[228:231], v[208:211], v[224:227]\n"
        "s_waitcnt lgkmcnt(6)\n"
        "v_mfma_f32_32x32x16_bf16 v[128:143], v[192:195], v[32:35], v[128:143]\n"
        "ds_read_b64_tr_b16 v[208:209], v241 offset:20480\n"
        "ds_read_b64_tr_b16 v[210:211], v241 offset:21504\n"
        "s_add_u32 s71, s71, 14336\n"
        "s_waitcnt lgkmcnt(6)\n"
        "v_mfma_f32_32x32x16_bf16 v[144:159], v[196:199], v[32:35], v[144:159]\n"
        "ds_read_b64_tr_b16 v[212:213], v242 offset:20480\n"
        "ds_read_b64_tr_b16 v[214:215], v242 offset:21504\n"
        "buffer_load_dwordx4 v240, s[44:47], s71 offen  lds\n"
        "s_waitcnt lgkmcnt(6)\n"
        "v_mfma_f32_32x32x16_bf16 v[160:175], v[200:203], v[32:35], v[160:175]\n"
        "ds_read_b64_tr_b16 v[216:217], v243 offset:20480\n"
        "ds_read_b64_tr_b16 v[218:219], v243 offset:21504\n"
        "s_add_u32 m0, s70, 18432\n"
        "s_waitcnt lgkmcnt(6)\n"
        "v_mfma_f32_32x32x16_bf16 v[176:191], v[204:207], v[32:35], v[176:191]\n"
        "ds_read_b64_tr_b16 v[220:221], v244 offset:20480\n"
        "ds_read_b64_tr_b16 v[222:223], v244 offset:21504\n"
        "s_add_u32 s71, s71, 14336\n"
        "v_mfma_f32_16x16x32_bf16 v[224:227], v[228:231], v[192:195], v[224:227]\n"
        "s_waitcnt lgkmcnt(6)\n"
        "v_mfma_f32_32x32x16_bf16 v[128:143], v[208:211], v[36:39], v[128:143]\n"
        "ds_read_b64_tr_b16 v[192:193], v241 offset:24576\n"
        "ds_read_b64_tr_b16 v[194:195], v241 offset:25600\n"
        "buffer_load_dwordx4 v240, s[44:47], s71 offen  lds\n"
        "s_waitcnt lgkmcnt(6)\n"
        "v_mfma_f32_32x32x16_bf16 v[144:159], v[212:215], v[36:39], v[144:159]\n"
        "ds_read_b64_tr_b16 v[196:197], v242 offset:24576\n"
        "ds_read_b64_tr_b16 v[198:199], v242 offset:25600\n"
        "s_waitcnt vmcnt(3)\n"
        "s_add_u32 s14, s14, 4\n"
        "s_mov_b64 exec, 1\n"
        "ds_add_u32 v246, v247\n"
        "s_mov_b64 exec, -1\n"
        "s_add_u32 m0, s70, 19456\n"
        "s_waitcnt lgkmcnt(7)\n"
        "v_mfma_f32_32x32x16_bf16 v[160:175], v[216:219], v[36:39], v[160:175]\n"
        "ds_read_b64_tr_b16 v[200:201], v243 offset:24576\n"
        "ds_read_b64_tr_b16 v[202:203], v243 offset:25600\n"
        "s_add_u32 s71, s71, 14336\n"
        "s_waitcnt lgkmcnt(7)\n"
        "v_mfma_f32_32x32x16_bf16 v[176:191], v[220:223], v[36:39], v[176:191]\n"
        "ds_read_b64_tr_b16 v[204:205], v244 offset:24576\n"
        "ds_read_b64_tr_b16 v[206:207], v244 offset:25600\n"
        "buffer_load_dwordx4 v240, s[44:47], s71 offen  lds\n"
        "v_mfma_f32_16x16x32_bf16 v[224:227], v[228:231], v[208:211], v[224:227]\n"
        "s_waitcnt lgkmcnt(7)\n"
        "v_mfma_f32_32x32x16_bf16 v[128:143], v[192:195], v[40:43], v[128:143]\n"
        "ds_read_b64_tr_b16 v[208:209], v241 offset:28672\n"
        "ds_read_b64_tr_b16 v[210:211], v241 offset:29696\n"
        "s_add_u32 m0, s70, 32768\n"
        "s_waitcnt lgkmcnt(7)\n"
        "v_mfma_f32_32x32x16_bf16 v[144:159], v[196:199], v[40:43], v[144:159]\n"
        "ds_read_b64_tr_b16 v[212:213], v242 offset:28672\n"
        "ds_read_b64_tr_b16 v[214:215], v242 offset:29696\n"
        "s_mul_i32 s71, s93, 229376\n"
        "s_waitcnt lgkmcnt(6)\n"
        "v_mfma_f32_32x32x16_bf16 v[160:175], v[200:203], v[40:43], v[160:175]\n"
        "ds_read_b64_tr_b16 v[216:217], v243 offset:28672\n"
        "ds_read_b64_tr_b16 v[218:219], v243 offset:29696\n"
        "buffer_load_dwordx4 v240, s[44:47], s71 offen  lds\n"
        "s_waitcnt lgkmcnt(6)\n"
        "v_mfma_f32_32x32x16_bf16 v[176:191], v[204:207], v[40:43], v[176:191]\n"
        "ds_read_b64_tr_b16 v[220:221], v244 offset:28672\n"
        "ds_read_b64_tr_b16 v[222:223], v244 offset:29696\n"
        "s_add_u32 m0, s70, 33792\n"
        "v_mfma_f32_16x16x32_bf16 v[224:227], v[228:231], v[192:195], v[224:227]\n"
        "ds_read_b32 v1, v246\n"
        "s_waitcnt lgkmcnt(7)\n"
        "v_mfma_f32_32x32x16_bf16 v[128:143], v[208:211], v[44:47], v[128:143]\n"
        "s_mov_b32 s26, 0\n"
        "s_waitcnt lgkmcnt(0)\n"
        "s_branch .Lsmid_31\n"
        ".Lspin_29:\n"
        "ds_read_b32 v1, v246\n"
        "s_waitcnt lgkmcnt(0)\n"
        ".Lsmid_31:\n"
        "s_setprio 1\n"
        "v_readfirstlane_b32 s27, v1\n"
        "s_setprio 0\n"
        "s_add_u32 s26, s26, 1\n"
        "s_and_b32 s25, s27, 0xffff\n"
        "s_cmp_ge_u32 s25, s14\n"
        "s_cbranch_scc1 .Lsdone_30\n"
        "s_cmp_lt_u32 s26, 400000\n"
        "s_cbranch_scc1 .Lspin_29\n"
        ".Lsdone_30:\n"
        "s_setprio 1\n"
        "v_subrev_u32 v241, 73728, v241\n"
        "v_subrev_u32 v242, 73728, v242\n"
        "v_subrev_u32 v243, 73728, v243\n"
        "v_subrev_u32 v244, 73728, v244\n"
        "s_setprio 0\n"
        "ds_read_b64_tr_b16 v[192:193], v241 offset:0\n"
        "ds_read_b64_tr_b16 v[194:195], v241 offset:1024\n"
        "s_add_u32 s71, s71, 14336\n"
        "v_mfma_f32_32x32x16_bf16 v[144:159], v[212:215], v[44:47], v[144:159]\n"
        "ds_read_b64_tr_b16 v[196:197], v242 offset:0\n"
        "ds_read_b64_tr_b16 v[198:199], v242 offset:1024\n"
        "buffer_load_dwordx4 v240, s[44:47], s71 offen  lds\n"
        "v_mfma_f32_32x32x16_bf16 v[160:175], v[216:219], v[44:47], v[160:175]\n"
        "ds_read_b64_tr_b16 v[200:201], v243 offset:0\n"
        "ds_read_b64_tr_b16 v[202:203], v243 offset:1024\n"
        "s_add_u32 m0, s70, 34816\n"
        "v_mfma_f32_32x32x16_bf16 v[176:191], v[220:223], v[44:47], v[176:191]\n"
        "ds_read_b64_tr_b16 v[204:205], v244 offset:0\n"
        "ds_read_b64_tr_b16 v[206:207], v244 offset:1024\n"
        "s_add_u32 s71, s71, 14336\n"
        "v_mfma_f32_16x16x32_bf16 v[224:227], v[228:231], v[208:211], v[224:227]\n"
        "s_waitcnt lgkmcnt(6)\n"
        "v_mfma_f32_32x32x16_bf16 v[128:143], v[192:195], v[48:51], v[128:143]\n"
        "ds_read_b64_tr_b16 v[208:209], v241 offset:4096\n"
        "ds_read_b64_tr_b16 v[210:211], v241 offset:5120\n"
        "buffer_load_dwordx4 v240, s[44:47], s71 offen  lds\n"
        "s_waitcnt lgkmcnt(6)\n"
        "v_mfma_f32_32x32x16_bf16 v[144:159], v[196:199], v[48:51], v[144:159]\n"
        "ds_read_b64_tr_b16 v[212:213], v242 offset:4096\n"
        "ds_read_b64_tr_b16 v[214:215], v242 offset:5120\n"
        "s_add_u32 m0, s70, 35840\n"
        "s_waitcnt lgkmcnt(6)\n"
        "v_mfma_f32_32x32x16_bf16 v[160:175], v[200:203], v[48:51], v[160:175]\n"
        "ds_read_b64_tr_b16 v[216:217], v243 offset:4096\n"
        "ds_read_b64_tr_b16 v[218:219], v243 offset:5120\n"
        "s_add_u32 s71, s71, 14336\n"
        "s_waitcnt lgkmcnt(6)\n"
        "v_mfma_f32_32x32x16_bf16 v[176:191], v[204:207], v[48:51], v[176:191]\n"
        "ds_read_b64_tr_b16 v[220:221], v244 offset:4096\n"
        "ds_read_b64_tr_b16 v[222:223], v244 offset:5120\n"
        "buffer_load_dwordx4 v240, s[44:47], s71 offen  lds\n"
        "v_mfma_f32_16x16x32_bf16 v[224:227], v[228:231], v[192:195], v[224:227]\n"
        "s_waitcnt lgkmcnt(6)\n"
        "v_mfma_f32_32x32x16_bf16 v[128:143], v[208:211], v[52:55], v[128:143]\n"
        "ds_read_b64_tr_b16 v[192:193], v241 offset:8192\n"
        "ds_read_b64_tr_b16 v[194:195], v241 offset:9216\n"
        "s_add_u32 m0, s70, 49152\n"
        "s_waitcnt lgkmcnt(6)\n"
        "v_mfma_f32_32x32x16_bf16 v[144:159], v[212:215], v[52:55], v[144:159]\n"
        "ds_read_b64_tr_b16 v[196:197], v242 offset:8192\n"
        "ds_read_b64_tr_b16 v[198:199], v242 offset:9216\n"
        "s_waitcnt vmcnt(4)\n"
        "s_add_u32 s14, s14, 4\n"
        "s_mov_b64 exec, 1\n"
        "ds_add_u32 v246, v247\n"
        "s_mov_b64 exec, -1\n"
        "s_mul_i32 s71, s94, 229376\n"
        "s_waitcnt lgkmcnt(7)\n"
        "v_mfma_f32_32x32x16_bf16 v[160:175], v[216:219], v[52:55], v[160:175]\n"
        "ds_read_b64_tr_b16 v[200:201], v243 offset:8192\n"
        "ds_read_b64_tr_b16 v[202:203], v243 offset:9216\n"
        "buffer_load_dwordx4 v240, s[44:47], s71 offen  lds\n"
        "s_waitcnt lgkmcnt(7)\n"
        "v_mfma_f32_32x32x16_bf16 v[176:191], v[220:223], v[52:55], v[176:191]\n"
        "ds_read_b64_tr_b16 v[204:205], v244 offset:8192\n"
        "ds_read_b64_tr_b16 v[206:207], v244 offset:9216\n"
        "s_add_u32 m0, s70, 50176\n"
        "v_mfma_f32_16x16x32_bf16 v[224:227], v[228:231], v[208:211], v[224:227]\n"
        "s_waitcnt lgkmcnt(7)\n"
        "v_mfma_f32_32x32x16_bf16 v[128:143], v[192:195], v[56:59], v[128:143]\n"
        "ds_read_b64_tr_b16 v[208:209], v241 offset:12288\n"
        "ds_read_b64_tr_b16 v[210:211], v241 offset:13312\n"
        "s_add_u32 s71, s71, 14336\n"
        "s_waitcnt lgkmcnt(7)\n"
        "v_mfma_f32_32x32x16_bf16 v[144:159], v[196:199], v[56:59], v[144:159]\n"
        "ds_read_b64_tr_b16 v[212:213], v242 offset:12288\n"
        "ds_read_b64_tr_b16 v[214:215], v242 offset:13312\n"
        "buffer_load_dwordx4 v240, s[44:47], s71 offen  lds\n"
        "s_waitcnt lgkmcnt(6)\n"
        "v_mfma_f32_32x32x16_bf16 v[160:175], v[200:203], v[56:59], v[160:175]\n"
        "ds_read_b64_tr_b16 v[216:217], v243 offset:12288\n"
        "ds_read_b64_tr_b16 v[218:219], v243 offset:13312\n"
        "s_add_u32 m0, s70, 51200\n"
        "s_waitcnt lgkmcnt(6)\n"
        "v_mfma_f32_32x32x16_bf16 v[176:191], v[204:207], v[56:59], v[176:191]\n"
        "ds_read_b64_tr_b16 v[220:221], v244 offset:12288\n"
        "ds_read_b64_tr_b16 v[222:223], v244 offset:13312\n"
        "s_add_u32 s71, s71, 14336\n"
        "v_mfma_f32_16x16x32_bf16 v[224:227], v[228:231], v[192:195], v[224:227]\n"
        "ds_read_b32 v1, v246\n"
        "s_waitcnt lgkmcnt(7)\n"
        "v_mfma_f32_32x32x16_bf16 v[128:143], v[208:211], v[60:63], v[128:143]\n"
        "s_mov_b32 s26, 0\n"
        "s_waitcnt lgkmcnt(0)\n"
        "s_branch .Lsmid_34\n"
        ".Lspin_32:\n"
        "ds_read_b32 v1, v246\n"
        "s_waitcnt lgkmcnt(0)\n"
        ".Lsmid_34:\n"
        "s_setprio 1\n"
        "v_readfirstlane_b32 s27, v1\n"
        "s_setprio 0\n"
        "s_add_u32 s26, s26, 1\n"
        "s_and_b32 s25, s27, 0xffff\n"
        "s_cmp_ge_u32 s25, s14\n"
        "s_cbranch_scc1 .Lsdone_33\n"
        "s_cmp_lt_u32 s26, 400000\n"
        "s_cbranch_scc1 .Lspin_32\n"
        ".Lsdone_33:\n"
        "ds_read_b64_tr_b16 v[192:193], v241 offset:16384\n"
        "ds_read_b64_tr_b16 v[194:195], v241 offset:17408\n"
        "buffer_load_dwordx4 v240, s[44:47], s71 offen  lds\n"
        "v_mfma_f32_32x32x16_bf16 v[144:159], v[212:215], v[60:63], v[144:159]\n"
        "ds_read_b64_tr_b16 v[196:197], v242 offset:16384\n"
        "ds_read_b64_tr_b16 v[198:199], v242 offset:17408\n"
        "s_add_u32 m0, s70, 52224\n"
        "v_mfma_f32_32x32x16_bf16 v[160:175], v[216:219], v[60:63], v[160:175]\n"
        "ds_read_b64_tr_b16 v[200:201], v243 offset:16384\n"
        "ds_read_b64_tr_b16 v[202:203], v243 offset:17408\n"
        "s_add_u32 s71, s71, 14336\n"
        "v_mfma_f32_32x32x16_bf16 v[176:191], v[220:223], v[60:63], v[176:191]\n"
        "ds_read_b64_tr_b16 v[204:205], v244 offset:16384\n"
        "ds_read_b64_tr_b16 v[206:207], v244 offset:17408\n"
        "buffer_load_dwordx4 v240, s[44:47], s71 offen  lds\n"
        "v_mfma_f32_16x16x32_bf16 v[224:227], v[228:231], v[208:211], v[224:227]\n"
        "s_waitcnt lgkmcnt(6)\n"
        "v_mfma_f32_32x32x16_bf16 v[128:143], v[192:195], v[64:67], v[128:143]\n"
        "ds_read_b64_tr_b16 v[208:209], v241 offset:20480\n"
        "ds_read_b64_tr_b16 v[210:211], v241 offset:21504\n"
        "s_add_u32 m0, s76, 65536\n"
        "s_waitcnt lgkmcnt(6)\n"
        "v_mfma_f32_32x32x16_bf16 v[144:159], v[196:199], v[64:67], v[144:159]\n"
        "ds_read_b64_tr_b16 v[212:213], v242 offset:20480\n"
        "ds_read_b64_tr_b16 v[214:215], v242 offset:21504\n"
        "s_mul_i32 s80, s77, 14336\n"
        "s_waitcnt lgkmcnt(6)\n"
        "v_mfma_f32_32x32x16_bf16 v[160:175], v[200:203], v[64:67], v[160:175]\n"
        "ds_read_b64_tr_b16 v[216:217], v243 offset:20480\n"
        "ds_read_b64_tr_b16 v[218:219], v243 offset:21504\n"
        "buffer_load_dwordx4 v232, s[52:55], s80 offen  lds\n"
        "s_waitcnt lgkmcnt(6)\n"
        "v_mfma_f32_32x32x16_bf16 v[176:191], v[204:207], v[64:67], v[176:191]\n"
        "ds_read_b64_tr_b16 v[220:221], v244 offset:20480\n"
        "ds_read_b64_tr_b16 v[222:223], v244 offset:21504\n"
        "s_add_u32 m0, s76, 66560\n"
        "v_mfma_f32_16x16x32_bf16 v[224:227], v[228:231], v[192:195], v[224:227]\n"
        "s_waitcnt lgkmcnt(6)\n"
        "v_mfma_f32_32x32x16_bf16 v[128:143], v[208:211], v[68:71], v[128:143]\n"
        "ds_read_b64_tr_b16 v[192:193], v241 offset:24576\n"
        "ds_read_b64_tr_b16 v[194:195], v241 offset:25600\n"
        "s_nop 0\n"
        "s_waitcnt lgkmcnt(6)\n"
        "v_mfma_f32_32x32x16_bf16 v[144:159], v[212:215], v[68:71], v[144:159]\n"
        "ds_read_b64_tr_b16 v[196:197], v242 offset:24576\n"
        "ds_read_b64_tr_b16 v[198:199], v242 offset:25600\n"
        "s_waitcnt vmcnt(5)\n"
        "s_add_u32 s14, s14, 4\n"
        "s_mov_b64 exec, 1\n"
        "ds_add_u32 v246, v247\n"
        "s_mov_b64 exec, -1\n"
        "buffer_load_dwordx4 v233, s[52:55], s80 offen  lds\n"
        "s_waitcnt lgkmcnt(7)\n"
        "v_mfma_f32_32x32x16_bf16 v[160:175], v[216:219], v[68:71], v[160:175]\n"
        "ds_read_b64_tr_b16 v[200:201], v243 offset:24576\n"
        "ds_read_b64_tr_b16 v[202:203], v243 offset:25600\n"
        "s_add_u32 s77, s77, 1\n"
        "s_cmp_eq_u32 s77, s78\n"
        "s_cselect_b32 s77, s79, s77\n"
        "s_waitcnt lgkmcnt(7)\n"
        "v_mfma_f32_32x32x16_bf16 v[176:191], v[220:223], v[68:71], v[176:191]\n"
        "ds_read_b64_tr_b16 v[204:205], v244 offset:24576\n"
        "ds_read_b64_tr_b16 v[206:207], v244 offset:25600\n"
        "s_add_u32 m0, s70, 73728\n"
        "v_mfma_f32_16x16x32_bf16 v[224:227], v[228:231], v[208:211], v[224:227]\n"
        "s_waitcnt lgkmcnt(7)\n"
        "v_mfma_f32_32x32x16_bf16 v[128:143], v[192:195], v[72:75], v[128:143]\n"
        "ds_read_b64_tr_b16 v[208:209], v241 offset:28672\n"
        "ds_read_b64_tr_b16 v[210:211], v241 offset:29696\n"
        "s_mul_i32 s71, s95, 229376\n"
        "s_waitcnt lgkmcnt(7)\n"
        "v_mfma_f32_32x32x16_bf16 v[144:159], v[196:199], v[72:75], v[144:159]\n"
        "ds_read_b64_tr_b16 v[212:213], v242 offset:28672\n"
        "ds_read_b64_tr_b16 v[214:215], v242 offset:29696\n"
        "buffer_load_dwordx4 v240, s[44:47], s71 offen  lds\n"
        "s_waitcnt lgkmcnt(6)\n"
        "v_mfma_f32_32x32x16_bf16 v[160:175], v[200:203], v[72:75], v[160:175]\n"
        "ds_read_b64_tr_b16 v[216:217], v243 offset:28672\n"
        "ds_read_b64_tr_b16 v[218:219], v243 offset:29696\n"
        "s_add_u32 m0, s70, 74752\n"
        "s_waitcnt lgkmcnt(6)\n"
        "v_mfma_f32_32x32x16_bf16 v[176:191], v[204:207], v[72:75], v[176:191]\n"
        "ds_read_b64_tr_b16 v[220:221], v244 offset:28672\n"
        "ds_read_b64_tr_b16 v[222:223], v244 offset:29696\n"
        "s_add_u32 s71, s71, 14336\n"
        "v_mfma_f32_16x16x32_bf16 v[224:227], v[228:231], v[192:195], v[224:227]\n"
        "ds_read_b32 v1, v246\n"
        "s_waitcnt lgkmcnt(7)\n"
        "v_mfma_f32_32x32x16_bf16 v[128:143], v[208:211], v[76:79], v[128:143]\n"
        "s_mov_b32 s26, 0\n"
        "s_waitcnt lgkmcnt(0)\n"
        "s_branch .Lsmid_37\n"
        ".Lspin_35:\n"
        "ds_read_b32 v1, v246\n"
        "s_waitcnt lgkmcnt(0)\n"
        ".Lsmid_37:\n"
        "s_setprio 1\n"
        "v_readfirstlane_b32 s27, v1\n"
        "s_setprio 0\n"
        "s_add_u32 s26, s26, 1\n"
        "s_and_b32 s25, s27, 0xffff\n"
        "s_cmp_ge_u32 s25, s14\n"
        "s_cbranch_scc1 .Lsdone_36\n"
        "s_cmp_lt_u32 s26, 400000\n"
        "s_cbranch_scc1 .Lspin_35\n"
        ".Lsdone_36:\n"
        "ds_read_b64_tr_b16 v[192:193], v241 offset:32768\n"
        "ds_read_b64_tr_b16 v[194:195], v241 offset:33792\n"
        "buffer_load_dwordx4 v240, s[44:47], s71 offen  lds\n"
        "v_mfma_f32_32x32x16_bf16 v[144:159], v[212:215], v[76:79], v[144:159]\n"
        "ds_read_b64_tr_b16 v[196:197], v242 offset:32768\n"
        "ds_read_b64_tr_b16 v[198:199], v242 offset:33792\n"
        "s_add_u32 m0, s70, 75776\n"
        "v_mfma_f32_32x32x16_bf16 v[160:175], v[216:219], v[76:79], v[160:175]\n"
        "ds_read_b64_tr_b16 v[200:201], v243 offset:32768\n"
        "ds_read_b64_tr_b16 v[202:203], v243 offset:33792\n"
        "s_add_u32 s71, s71, 14336\n"
        "v_mfma_f32_32x32x16_bf16 v[176:191], v[220:223], v[76:79], v[176:191]\n"
        "ds_read_b64_tr_b16 v[204:205], v244 offset:32768\n"
        "ds_read_b64_tr_b16 v[206:207], v244 offset:33792\n"
        "buffer_load_dwordx4 v240, s[44:47], s71 offen  lds\n"
        "v_mfma_f32_16x16x32_bf16 v[224:227], v[228:231], v[208:211], v[224:227]\n"
        "s_waitcnt lgkmcnt(6)\n"
        "v_mfma_f32_32x32x16_bf16 v[128:143], v[192:195], v[80:83], v[128:143]\n"
        "ds_read_b64_tr_b16 v[208:209], v241 offset:36864\n"
        "ds_read_b64_tr_b16 v[210:211], v241 offset:37888\n"
        "s_add_u32 m0, s70, 76800\n"
        "s_waitcnt lgkmcnt(6)\n"
        "v_mfma_f32_32x32x16_bf16 v[144:159], v[196:199], v[80:83], v[144:159]\n"
        "ds_read_b64_tr_b16 v[212:213], v242 offset:36864\n"
        "ds_read_b64_tr_b16 v[214:215], v242 offset:37888\n"
        "s_add_u32 s71, s71, 14336\n"
        "s_waitcnt lgkmcnt(6)\n"
        "v_mfma_f32_32x32x16_bf16 v[160:175], v[200:203], v[80:83], v[160:175]\n"
        "ds_read_b64_tr_b16 v[216:217], v243 offset:36864\n"
        "ds_read_b64_tr_b16 v[218:219], v243 offset:37888\n"
        "buffer_load_dwordx4 v240, s[44:47], s71 offen  lds\n"
        "s_waitcnt lgkmcnt(6)\n"
        "v_mfma_f32_32x32x16_bf16 v[176:191], v[204:207], v[80:83], v[176:191]\n"
        "ds_read_b64_tr_b16 v[220:221], v244 offset:36864\n"
        "ds_read_b64_tr_b16 v[222:223], v244 offset:37888\n"
        "s_add_u32 m0, s76, 90112\n"
        "v_mfma_f32_16x16x32_bf16 v[224:227], v[228:231], v[192:195], v[224:227]\n"
        "s_waitcnt lgkmcnt(6)\n"
        "v_mfma_f32_32x32x16_bf16 v[128:143], v[208:211], v[84:87], v[128:143]\n"
        "ds_read_b64_tr_b16 v[192:193], v241 offset:40960\n"
        "ds_read_b64_tr_b16 v[194:195], v241 offset:41984\n"
        "s_mul_i32 s80, s77, 14336\n"
        "s_waitcnt lgkmcnt(6)\n"
        "v_mfma_f32_32x32x16_bf16 v[144:159], v[212:215], v[84:87], v[144:159]\n"
        "ds_read_b64_tr_b16 v[196:197], v242 offset:40960\n"
        "ds_read_b64_tr_b16 v[198:199], v242 offset:41984\n"
        "s_waitcnt vmcnt(6)\n"
        "s_add_u32 s14, s14, 4\n"
        "s_mov_b64 exec, 1\n"
        "ds_add_u32 v246, v247\n"
        "s_mov_b64 exec, -1\n"
        "buffer_load_dwordx4 v232, s[52:55], s80 offen  lds\n"
        "s_waitcnt lgkmcnt(7)\n"
        "v_mfma_f32_32x32x16_bf16 v[160:175], v[216:219], v[84:87], v[160:175]\n"
        "ds_read_b64_tr_b16 v[200:201], v243 offset:40960\n"
        "ds_read_b64_tr_b16 v[202:203], v243 offset:41984\n"
        "s_add_u32 m0, s76, 91136\n"
        "s_waitcnt lgkmcnt(7)\n"
        "v_mfma_f32_32x32x16_bf16 v[176:191], v[220:223], v[84:87], v[176:191]\n"
        "ds_read_b64_tr_b16 v[204:205], v244 offset:40960\n"
        "ds_read_b64_tr_b16 v[206:207], v244 offset:41984\n"
        "s_nop 0\n"
        "v_mfma_f32_16x16x32_bf16 v[224:227], v[228:231], v[208:211], v[224:227]\n"
        "s_waitcnt lgkmcnt(7)\n"
        "v_mfma_f32_32x32x16_bf16 v[128:143], v[192:195], v[88:91], v[128:143]\n"
        "ds_read_b64_tr_b16 v[208:209], v241 offset:45056\n"
        "ds_read_b64_tr_b16 v[210:211], v241 offset:46080\n"
        "buffer_load_dwordx4 v233, s[52:55], s80 offen  lds\n"
        "s_waitcnt lgkmcnt(7)\n"
        "v_mfma_f32_32x32x16_bf16 v[144:159], v[196:199], v[88:91], v[144:159]\n"
        "ds_read_b64_tr_b16 v[212:213], v242 offset:45056\n"
        "ds_read_b64_tr_b16 v[214:215], v242 offset:46080\n"
        "s_add_u32 s77, s77, 1\n"
        "s_cmp_eq_u32 s77, s78\n"
        "s_cselect_b32 s77, s79, s77\n"
        "s_waitcnt lgkmcnt(6)\n"
        "v_mfma_f32_32x32x16_bf16 v[160:175], v[200:203], v[88:91], v[160:175]\n"
        "ds_read_b64_tr_b16 v[216:217], v243 offset:45056\n"
        "ds_read_b64_tr_b16 v[218:219], v243 offset:46080\n"
        "s_add_u32 m0, s76, 98304\n"
        "s_waitcnt lgkmcnt(6)\n"
        "v_mfma_f32_32x32x16_bf16 v[176:191], v[204:207], v[88:91], v[176:191]\n"
        "ds_read_b64_tr_b16 v[220:221], v244 offset:45056\n"
        "ds_read_b64_tr_b16 v[222:223], v244 offset:46080\n"
        "s_mul_i32 s80, s77, 14336\n"
        "v_mfma_f32_16x16x32_bf16 v[224:227], v[228:231], v[192:195], v[224:227]\n"
        "ds_read_b32 v1, v246\n"
        "s_waitcnt lgkmcnt(7)\n"
        "v_mfma_f32_32x32x16_bf16 v[128:143], v[208:211], v[92:95], v[128:143]\n"
        "s_mov_b32 s26, 0\n"
        "s_waitcnt lgkmcnt(0)\n"
        "s_branch .Lsmid_40\n"
        ".Lspin_38:\n"
        "ds_read_b32 v1, v246\n"
        "s_waitcnt lgkmcnt(0)\n"
        ".Lsmid_40:\n"
        "s_setprio 1\n"
        "v_readfirstlane_b32 s27, v1\n"
        "s_setprio 0\n"
        "s_add_u32 s26, s26, 1\n"
        "s_and_b32 s25, s27, 0xffff\n"
        "s_cmp_ge_u32 s25, s14\n"
        "s_cbranch_scc1 .Lsdone_39\n"
        "s_cmp_lt_u32 s26, 400000\n"
        "s_cbranch_scc1 .Lspin_38\n"
        ".Lsdone_39:\n"
        "ds_read_b64_tr_b16 v[192:193], v241 offset:49152\n"
        "ds_read_b64_tr_b16 v[194:195], v241 offset:50176\n"
        "buffer_load_dwordx4 v232, s[52:55], s80 offen  lds\n"
        "v_mfma_f32_32x32x16_bf16 v[144:159], v[212:215], v[92:95], v[144:159]\n"
        "ds_read_b64_tr_b16 v[196:197], v242 offset:49152\n"
        "ds_read_b64_tr_b16 v[198:199], v242 offset:50176\n"
        "s_add_u32 m0, s76, 99328\n"
        "v_mfma_f32_32x32x16_bf16 v[160:175], v[216:219], v[92:95], v[160:175]\n"
        "ds_read_b64_tr_b16 v[200:201], v243 offset:49152\n"
        "ds_read_b64_tr_b16 v[202:203], v243 offset:50176\n"
        "s_nop 0\n"
        "v_mfma_f32_32x32x16_bf16 v[176:191], v[220:223], v[92:95], v[176:191]\n"
        "ds_read_b64_tr_b16 v[204:205], v244 offset:49152\n"
        "ds_read_b64_tr_b16 v[206:207], v244 offset:50176\n"
        "buffer_load_dwordx4 v233, s[52:55], s80 offen  lds\n"
        "v_mfma_f32_16x16x32_bf16 v[224:227], v[228:231], v[208:211], v[224:227]\n"
        "s_waitcnt lgkmcnt(6)\n"
        "v_mfma_f32_32x32x16_bf16 v[128:143], v[192:195], v[96:99], v[128:143]\n"
        "ds_read_b64_tr_b16 v[208:209], v241 offset:53248\n"
        "ds_read_b64_tr_b16 v[210:211], v241 offset:54272\n"
        "s_add_u32 s77, s77, 1\n"
        "s_cmp_eq_u32 s77, s78\n"
        "s_cselect_b32 s77, s79, s77\n"
        "s_waitcnt lgkmcnt(6)\n"
        "v_mfma_f32_32x32x16_bf16 v[144:159], v[196:199], v[96:99], v[144:159]\n"
        "ds_read_b64_tr_b16 v[212:213], v242 offset:53248\n"
        "ds_read_b64_tr_b16 v[214:215], v242 offset:54272\n"
        "s_add_u32 m0, s76, 0\n"
        "s_waitcnt lgkmcnt(6)\n"
        "v_mfma_f32_32x32x16_bf16 v[160:175], v[200:203], v[96:99], v[160:175]\n"
        "ds_read_b64_tr_b16 v[216:217], v243 offset:53248\n"
        "ds_read_b64_tr_b16 v[218:219], v243 offset:54272\n"
        "s_mul_i32 s80, s77, 14336\n"
        "s_waitcnt lgkmcnt(6)\n"
        "v_mfma_f32_32x32x16_bf16 v[176:191], v[204:207], v[96:99], v[176:191]\n"
        "ds_read_b64_tr_b16 v[220:221], v244 offset:53248\n"
        "ds_read_b64_tr_b16 v[222:223], v244 offset:54272\n"
        "buffer_load_dwordx4 v232, s[52:55], s80 offen  lds\n"
        "v_mfma_f32_16x16x32_bf16 v[224:227], v[228:231], v[192:195], v[224:227]\n"
        "s_waitcnt lgkmcnt(6)\n"
        "v_mfma_f32_32x32x16_bf16 v[128:143], v[208:211], v[100:103], v[128:143]\n"
        "ds_read_b64_tr_b16 v[192:193], v241 offset:57344\n"
        "ds_read_b64_tr_b16 v[194:195], v241 offset:58368\n"
        "s_add_u32 m0, s76, 1024\n"
        "s_waitcnt lgkmcnt(6)\n"
        "v_mfma_f32_32x32x16_bf16 v[144:159], v[212:215], v[100:103], v[144:159]\n"
        "ds_read_b64_tr_b16 v[196:197], v242 offset:57344\n"
        "ds_read_b64_tr_b16 v[198:199], v242 offset:58368\n"
        "s_waitcnt vmcnt(5)\n"
        "s_add_u32 s14, s14, 4\n"
        "s_mov_b64 exec, 1\n"
        "ds_add_u32 v246, v247\n"
        "s_mov_b64 exec, -1\n"
        "s_nop 0\n"
        "s_waitcnt lgkmcnt(7)\n"
        "v_mfma_f32_32x32x16_bf16 v[160:175], v[216:219], v[100:103], v[160:175]\n"
        "ds_read_b64_tr_b16 v[200:201], v243 offset:57344\n"
        "ds_read_b64_tr_b16 v[202:203], v243 offset:58368\n"
        "buffer_load_dwordx4 v233, s[52:55], s80 offen  lds\n"
        "s_waitcnt lgkmcnt(7)\n"
        "v_mfma_f32_32x32x16_bf16 v[176:191], v[220:223], v[100:103], v[176:191]\n"
        "ds_read_b64_tr_b16 v[204:205], v244 offset:57344\n"
        "ds_read_b64_tr_b16 v[206:207], v244 offset:58368\n"
        "s_add_u32 s77, s77, 1\n"
        "s_cmp_eq_u32 s77, s78\n"
        "s_cselect_b32 s77, s79, s77\n"
        "v_mfma_f32_16x16x32_bf16 v[224:227], v[228:231], v[208:211], v[224:227]\n"
        "s_waitcnt lgkmcnt(7)\n"
        "v_mfma_f32_32x32x16_bf16 v[128:143], v[192:195], v[104:107], v[128:143]\n"
        "ds_read_b64_tr_b16 v[208:209], v241 offset:61440\n"
        "ds_read_b64_tr_b16 v[210:211], v241 offset:62464\n"
        "s_add_u32 m0, s76, 8192\n"
        "s_waitcnt lgkmcnt(7)\n"
        "v_mfma_f32_32x32x16_bf16 v[144:159], v[196:199], v[104:107], v[144:159]\n"
        "ds_read_b64_tr_b16 v[212:213], v242 offset:61440\n"
        "ds_read_b64_tr_b16 v[214:215], v242 offset:62464\n"
        "s_mul_i32 s80, s77, 14336\n"
        "s_waitcnt lgkmcnt(6)\n"
        "v_mfma_f32_32x32x16_bf16 v[160:175], v[200:203], v[104:107], v[160:175]\n"
        "ds_read_b64_tr_b16 v[216:217], v243 offset:61440\n"
        "ds_read_b64_tr_b16 v[218:219], v243 offset:62464\n"
        "buffer_load_dwordx4 v232, s[52:55], s80 offen  lds\n"
        "s_waitcnt lgkmcnt(6)\n"
        "v_mfma_f32_32x32x16_bf16 v[176:191], v[204:207], v[104:107], v[176:191]\n"
        "ds_read_b64_tr_b16 v[220:221], v244 offset:61440\n"
        "ds_read_b64_tr_b16 v[222:223], v244 offset:62464\n"
        "s_add_u32 m0, s76, 9216\n"
        "v_mfma_f32_16x16x32_bf16 v[224:227], v[228:231], v[192:195], v[224:227]\n"
        "ds_read_b32 v1, v246\n"
        "s_waitcnt lgkmcnt(7)\n"
        "v_mfma_f32_32x32x16_bf16 v[128:143], v[208:211], v[108:111], v[128:143]\n"
        "s_mov_b32 s26, 0\n"
        "s_waitcnt lgkmcnt(0)\n"
        "s_branch .Lsmid_43\n"
        ".Lspin_41:\n"
        "ds_read_b32 v1, v246\n"
        "s_waitcnt lgkmcnt(0)\n"
        ".Lsmid_43:\n"
        "s_setprio 1\n"
        "v_readfirstlane_b32 s27, v1\n"
        "s_setprio 0\n"
        "s_add_u32 s26, s26, 1\n"
        "s_and_b32 s25, s27, 0xffff\n"
        "s_cmp_ge_u32 s25, s14\n"
        "s_cbranch_scc1 .Lsdone_42\n"
        "s_cmp_lt_u32 s26, 400000\n"
        "s_cbranch_scc1 .Lspin_41\n"
        ".Lsdone_42:\n"
        "s_setprio 1\n"
        "v_add_u32 v241, 73728, v241\n"
        "v_add_u32 v242, 73728, v242\n"
        "v_add_u32 v243, 73728, v243\n"
        "v_add_u32 v244, 73728, v244\n"
        "s_setprio 0\n"
        "ds_read_b64_tr_b16 v[192:193], v241 offset:0\n"
        "ds_read_b64_tr_b16 v[194:195], v241 offset:1024\n"
        "s_nop 0\n"
        "v_mfma_f32_32x32x16_bf16 v[144:159], v[212:215], v[108:111], v[144:159]\n"
        "ds_read_b64_tr_b16 v[196:197], v242 offset:0\n"
        "ds_read_b64_tr_b16 v[198:199], v242 offset:1024\n"
        "buffer_load_dwordx4 v233, s[52:55], s80 offen  lds\n"
        "v_mfma_f32_32x32x16_bf16 v[160:175], v[216:219], v[108:111], v[160:175]\n"
        "ds_read_b64_tr_b16 v[200:201], v243 offset:0\n"
        "ds_read_b64_tr_b16 v[202:203], v243 offset:1024\n"
        "s_add_u32 s77, s77, 1\n"
        "s_cmp_eq_u32 s77, s78\n"
        "s_cselect_b32 s77, s79, s77\n"
        "v_mfma_f32_32x32x16_bf16 v[176:191], v[220:223], v[108:111], v[176:191]\n"
        "ds_read_b64_tr_b16 v[204:205], v244 offset:0\n"
        "ds_read_b64_tr_b16 v[206:207], v244 offset:1024\n"
        "s_add_u32 m0, s76, 16384\n"
        "v_mfma_f32_16x16x32_bf16 v[224:227], v[228:231], v[208:211], v[224:227]\n"
        "s_waitcnt lgkmcnt(6)\n"
        "v_mfma_f32_32x32x16_bf16 v[128:143], v[192:195], v[112:115], v[128:143]\n"
        "ds_read_b64_tr_b16 v[208:209], v241 offset:4096\n"
        "ds_read_b64_tr_b16 v[210:211], v241 offset:5120\n"
        "s_mul_i32 s80, s77, 14336\n"
        "s_waitcnt lgkmcnt(6)\n"
        "v_mfma_f32_32x32x16_bf16 v[144:159], v[196:199], v[112:115], v[144:159]\n"
        "ds_read_b64_tr_b16 v[212:213], v242 offset:4096\n"
        "ds_read_b64_tr_b16 v[214:215], v242 offset:5120\n"
        "buffer_load_dwordx4 v232, s[52:55], s80 offen  lds\n"
        "s_waitcnt lgkmcnt(6)\n"
        "v_mfma_f32_32x32x16_bf16 v[160:175], v[200:203], v[112:115], v[160:175]\n"
        "ds_read_b64_tr_b16 v[216:217], v243 offset:4096\n"
        "ds_read_b64_tr_b16 v[218:219], v243 offset:5120\n"
        "s_add_u32 m0, s76, 17408\n"
        "s_waitcnt lgkmcnt(6)\n"
        "v_mfma_f32_32x32x16_bf16 v[176:191], v[204:207], v[112:115], v[176:191]\n"
        "ds_read_b64_tr_b16 v[220:221], v244 offset:4096\n"
        "ds_read_b64_tr_b16 v[222:223], v244 offset:5120\n"
        "s_nop 0\n"
        "v_mfma_f32_16x16x32_bf16 v[224:227], v[228:231], v[192:195], v[224:227]\n"
        "s_waitcnt lgkmcnt(6)\n"
        "v_mfma_f32_32x32x16_bf16 v[128:143], v[208:211], v[116:119], v[128:143]\n"
        "ds_read_b64_tr_b16 v[192:193], v241 offset:8192\n"
        "ds_read_b64_tr_b16 v[194:195], v241 offset:9216\n"
        "buffer_load_dwordx4 v233, s[52:55], s80 offen  lds\n"
        "s_waitcnt lgkmcnt(6)\n"
        "v_mfma_f32_32x32x16_bf16 v[144:159], v[212:215], v[116:119], v[144:159]\n"
        "ds_read_b64_tr_b16 v[196:197], v242 offset:8192\n"
        "ds_read_b64_tr_b16 v[198:199], v242 offset:9216\n"
        "s_add_u32 s77, s77, 1\n"
        "s_cmp_eq_u32 s77, s78\n"
        "s_cselect_b32 s77, s79, s77\n"
        "s_waitcnt lgkmcnt(6)\n"
        "v_mfma_f32_32x32x16_bf16 v[160:175], v[216:219], v[116:119], v[160:175]\n"
        "ds_read_b64_tr_b16 v[200:201], v243 offset:8192\n"
        "ds_read_b64_tr_b16 v[202:203], v243 offset:9216\n"
        "s_add_u32 m0, s76, 24576\n"
        "s_waitcnt lgkmcnt(6)\n"
        "v_mfma_f32_32x32x16_bf16 v[176:191], v[220:223], v[116:119], v[176:191]\n"
        "ds_read_b64_tr_b16 v[204:205], v244 offset:8192\n"
        "ds_read_b64_tr_b16 v[206:207], v244 offset:9216\n"
        "s_mul_i32 s80, s77, 14336\n"
        "v_mfma_f32_16x16x32_bf16 v[224:227], v[228:231], v[208:211], v[224:227]\n"
        "s_waitcnt lgkmcnt(6)\n"
        "v_mfma_f32_32x32x16_bf16 v[128:143], v[192:195], v[120:123], v[128:143]\n"
        "ds_read_b64_tr_b16 v[208:209], v241 offset:12288\n"
        "ds_read_b64_tr_b16 v[210:211], v241 offset:13312\n"
        "buffer_load_dwordx4 v232, s[52:55], s80 offen  lds\n"
        "s_waitcnt lgkmcnt(6)\n"
        "v_mfma_f32_32x32x16_bf16 v[144:159], v[196:199], v[120:123], v[144:159]\n"
        "ds_read_b64_tr_b16 v[212:213], v242 offset:12288\n"
        "ds_read_b64_tr_b16 v[214:215], v242 offset:13312\n"
        "s_add_u32 m0, s76, 25600\n"
        "s_waitcnt lgkmcnt(6)\n"
        "v_mfma_f32_32x32x16_bf16 v[160:175], v[200:203], v[120:123], v[160:175]\n"
        "ds_read_b64_tr_b16 v[216:217], v243 offset:12288\n"
        "ds_read_b64_tr_b16 v[218:219], v243 offset:13312\n"
        "s_nop 0\n"
        "s_waitcnt lgkmcnt(6)\n"
        "v_mfma_f32_32x32x16_bf16 v[176:191], v[204:207], v[120:123], v[176:191]\n"
        "ds_read_b64_tr_b16 v[220:221], v244 offset:12288\n"
        "ds_read_b64_tr_b16 v[222:223], v244 offset:13312\n"
        "buffer_load_dwordx4 v233, s[52:55], s80 offen  lds\n"
        "v_mfma_f32_16x16x32_bf16 v[224:227], v[228:231], v[192:195], v[224:227]\n"
        "s_waitcnt lgkmcnt(6)\n"
        "v_mfma_f32_32x32x16_bf16 v[128:143], v[208:211], v[124:127], v[128:143]\n"
        "s_add_u32 s77, s77, 1\n"
        "s_cmp_eq_u32 s77, s78\n"
        "s_cselect_b32 s77, s79, s77\n"
        "s_waitcnt lgkmcnt(4)\n"
        "v_mfma_f32_32x32x16_bf16 v[144:159], v[212:215], v[124:127], v[144:159]\n"
        "s_add_u32 m0, s76, 32768\n"
        "s_waitcnt lgkmcnt(2)\n"
        "v_mfma_f32_32x32x16_bf16 v[160:175], v[216:219], v[124:127], v[160:175]\n"
        "s_mul_i32 s80, s77, 14336\n"
        "s_waitcnt lgkmcnt(0)\n"
        "v_mfma_f32_32x32x16_bf16 v[176:191], v[220:223], v[124:127], v[176:191]\n"
        "buffer_load_dwordx4 v232, s[52:55], s80 offen  lds\n"
        "v_mfma_f32_16x16x32_bf16 v[224:227], v[228:231], v[208:211], v[224:227]\n"
        "s_add_u32 m0, s76, 33792\n"
        "s_nop 0\n"
        "buffer_load_dwordx4 v233, s[52:55], s80 offen  lds\n"
        "s_add_u32 s77, s77, 1\n"
        "s_cmp_eq_u32 s77, s78\n"
        "s_cselect_b32 s77, s79, s77\n"
        "s_add_u32 m0, s76, 40960\n"
        "s_mul_i32 s80, s77, 14336\n"
        "buffer_load_dwordx4 v232, s[52:55], s80 offen  lds\n"
        "s_add_u32 m0, s76, 41984\n"
        "s_nop 0\n"
        "buffer_load_dwordx4 v233, s[52:55], s80 offen  lds\n"
        "s_add_u32 s77, s77, 1\n"
        "s_cmp_eq_u32 s77, s78\n"
        "s_cselect_b32 s77, s79, s77\n"
        "s_setprio 1\n"
        "s_lshl_b32 s18, s10, 8\n"
        "s_add_u32 s18, s18, 162816\n"
        "v_lshlrev_b32 v2, 2, v0\n"
        "v_add_u32 v1, s18, v2\n"
        "s_nop 3\n"
        "ds_write_b32 v1, v224\n"
        "s_add_u32 s14, s14, 4\n"
        "s_mov_b64 exec, 1\n"
        "ds_add_u32 v246, v247\n"
        "s_mov_b64 exec, -1\n"
        "s_mov_b32 s26, 0\n"
        ".Lspin_44:\n"
        "ds_read_b32 v1, v246\n"
        "s_waitcnt lgkmcnt(0)\n"
        ".Lsmid_46:\n"
        "v_readfirstlane_b32 s27, v1\n"
        "s_add_u32 s26, s26, 1\n"
        "s_and_b32 s25, s27, 0xffff\n"
        "s_cmp_ge_u32 s25, s14\n"
        "s_cbranch_scc1 .Lsdone_45\n"
        "s_cmp_lt_u32 s26, 400000\n"
        "s_cbranch_scc1 .Lspin_44\n"
        ".Lsdone_45:\n"
        "s_add_u32 s18, s10, 0\n"
        "s_and_b32 s18, s18, 3\n"
        "s_lshl_b32 s18, s18, 8\n"
        "s_add_u32 s18, s18, 162816\n"
        "v_add_u32 v1, s18, v2\n"
        "ds_read_b32 v4, v1\n"
        "v_mov_b32 v195, 0\n"
        "s_add_u32 s18, s10, 1\n"
        "s_and_b32 s18, s18, 3\n"
        "s_lshl_b32 s18, s18, 8\n"
        "s_add_u32 s18, s18, 162816\n"
        "v_add_u32 v1, s18, v2\n"
        "ds_read_b32 v5, v1\n"
        "v_mov_b32 v199, 0\n"
        "s_add_u32 s18, s10, 2\n"
        "s_and_b32 s18, s18, 3\n"
        "s_lshl_b32 s18, s18, 8\n"
        "s_add_u32 s18, s18, 162816\n"
        "v_add_u32 v1, s18, v2\n"
        "ds_read_b32 v6, v1\n"
        "v_mov_b32 v203, 0\n"
        "s_add_u32 s18, s10, 3\n"
        "s_and_b32 s18, s18, 3\n"
        "s_lshl_b32 s18, s18, 8\n"
        "s_add_u32 s18, s18, 162816\n"
        "v_add_u32 v1, s18, v2\n"
        "ds_read_b32 v7, v1\n"
        "v_mov_b32 v207, 0\n"
        "s_waitcnt lgkmcnt(0)\n"
        "v_cvt_pk_bf16_f32 v1, v4, v4\n"
        "v_and_b32 v2, 0xffff0000, v1\n"
        "v_sub_f32 v2, v4, v2\n"
        "v_cvt_pk_bf16_f32 v192, v4, v2\n"
        "v_and_b32 v1, 0xffff, v1\n"
        "v_lshl_or_b32 v193, v252, 16, v1\n"
        "v_mov_b32 v194, v252\n"
        "v_cvt_pk_bf16_f32 v1, v5, v5\n"
        "v_and_b32 v2, 0xffff0000, v1\n"
        "v_sub_f32 v2, v5, v2\n"
        "v_cvt_pk_bf16_f32 v196, v5, v2\n"
        "v_and_b32 v1, 0xffff, v1\n"
        "v_lshl_or_b32 v197, v253, 16, v1\n"
        "v_mov_b32 v198, v253\n"
        "v_cvt_pk_bf16_f32 v1, v6, v6\n"
        "v_and_b32 v2, 0xffff0000, v1\n"
        "v_sub_f32 v2, v6, v2\n"
        "v_cvt_pk_bf16_f32 v200, v6, v2\n"
        "v_and_b32 v1, 0xffff, v1\n"
        "v_lshl_or_b32 v201, v254, 16, v1\n"
        "v_mov_b32 v202, v254\n"
        "v_cvt_pk_bf16_f32 v1, v7, v7\n"
        "v_and_b32 v2, 0xffff0000, v1\n"
        "v_sub_f32 v2, v7, v2\n"
        "v_cvt_pk_bf16_f32 v204, v7, v2\n"
        "v_and_b32 v1, 0xffff, v1\n"
        "v_lshl_or_b32 v205, v255, 16, v1\n"
        "v_mov_b32 v206, v255\n"
        "s_nop 1\n"
        "v_mfma_f32_32x32x16_bf16 v[128:143], v[192:195], v[8:11], v[128:143]\n"
        "v_mfma_f32_32x32x16_bf16 v[144:159], v[196:199], v[8:11], v[144:159]\n"
        "v_mfma_f32_32x32x16_bf16 v[160:175], v[200:203], v[8:11], v[160:175]\n"
        "v_mfma_f32_32x32x16_bf16 v[176:191], v[204:207], v[8:11], v[176:191]\n"
        "s_mul_i32 s20, s72, 112\n"
        "s_lshl_b32 s19, s8, 3\n"
        "s_add_u32 s20, s20, s19\n"
        "s_lshl_b32 s20, s20, 10\n"
        "s_add_u32 s20, s20, 6291456\n"
        "s_nop 3\n"
        "s_add_u32 s18, s10, 0\n"
        "s_and_b32 s18, s18, 3\n"
        "s_lshl_b32 s18, s18, 11\n"
        "s_add_u32 s18, s18, s20\n"
        "v_pk_mul_f32 v[128:129], v[128:129], v[12:13] op_sel_hi:[1,0]\n"
        "v_max_f32 v2, 0, v128\n"
        "v_max_f32 v3, 0, v129\n"
        "v_pk_mul_f32 v[128:129], v[2:3], v[2:3]\n"
        "v_cvt_pk_bf16_f32 v16, v128, v129\n"
        "v_pk_mul_f32 v[130:131], v[130:131], v[12:13] op_sel_hi:[1,0]\n"
        "v_max_f32 v2, 0, v130\n"
        "v_max_f32 v3, 0, v131\n"
        "v_pk_mul_f32 v[130:131], v[2:3], v[2:3]\n"
        "v_cvt_pk_bf16_f32 v17, v130, v131\n"
        "v_pk_mul_f32 v[132:133], v[132:133], v[12:13] op_sel_hi:[1,0]\n"
        "v_max_f32 v2, 0, v132\n"
        "v_max_f32 v3, 0, v133\n"
        "v_pk_mul_f32 v[132:133], v[2:3], v[2:3]\n"
        "v_cvt_pk_bf16_f32 v18, v132, v133\n"
        "v_pk_mul_f32 v[134:135], v[134:135], v[12:13] op_sel_hi:[1,0]\n"
        "v_max_f32 v2, 0, v134\n"
        "v_max_f32 v3, 0, v135\n"
        "v_pk_mul_f32 v[134:135], v[2:3], v[2:3]\n"
        "v_cvt_pk_bf16_f32 v19, v134, v135\n"
        "buffer_store_dwordx4 v[16:19], v245, s[64:67], s18 offen offset:0 sc1\n"
        "v_pk_mul_f32 v[136:137], v[136:137], v[12:13] op_sel_hi:[1,0]\n"
        "v_max_f32 v2, 0, v136\n"
        "v_max_f32 v3, 0, v137\n"
        "v_pk_mul_f32 v[136:137], v[2:3], v[2:3]\n"
        "v_cvt_pk_bf16_f32 v20, v136, v137\n"
        "v_pk_mul_f32 v[138:139], v[138:139], v[12:13] op_sel_hi:[1,0]\n"
        "v_max_f32 v2, 0, v138\n"
        "v_max_f32 v3, 0, v139\n"
        "v_pk_mul_f32 v[138:139], v[2:3], v[2:3]\n"
        "v_cvt_pk_bf16_f32 v21, v138, v139\n"
        "v_pk_mul_f32 v[140:141], v[140:141], v[12:13] op_sel_hi:[1,0]\n"
        "v_max_f32 v2, 0, v140\n"
        "v_max_f32 v3, 0, v141\n"
        "v_pk_mul_f32 v[140:141], v[2:3], v[2:3]\n"
        "v_cvt_pk_bf16_f32 v22, v140, v141\n"
        "v_pk_mul_f32 v[142:143], v[142:143], v[12:13] op_sel_hi:[1,0]\n"
        "v_max_f32 v2, 0, v142\n"
        "v_max_f32 v3, 0, v143\n"
        "v_pk_mul_f32 v[142:143], v[2:3], v[2:3]\n"
        "v_cvt_pk_bf16_f32 v23, v142, v143\n"
        "buffer_store_dwordx4 v[20:23], v245, s[64:67], s18 offen offset:1024 sc1\n"
        "s_add_u32 s18, s10, 1\n"
        "s_and_b32 s18, s18, 3\n"
        "s_lshl_b32 s18, s18, 11\n"
        "s_add_u32 s18, s18, s20\n"
        "v_pk_mul_f32 v[144:145], v[144:145], v[12:13] op_sel_hi:[1,0]\n"
        "v_max_f32 v2, 0, v144\n"
        "v_max_f32 v3, 0, v145\n"
        "v_pk_mul_f32 v[144:145], v[2:3], v[2:3]\n"
        "v_cvt_pk_bf16_f32 v24, v144, v145\n"
        "v_pk_mul_f32 v[146:147], v[146:147], v[12:13] op_sel_hi:[1,0]\n"
        "v_max_f32 v2, 0, v146\n"
        "v_max_f32 v3, 0, v147\n"
        "v_pk_mul_f32 v[146:147], v[2:3], v[2:3]\n"
        "v_cvt_pk_bf16_f32 v25, v146, v147\n"
        "v_pk_mul_f32 v[148:149], v[148:149], v[12:13] op_sel_hi:[1,0]\n"
        "v_max_f32 v2, 0, v148\n"
        "v_max_f32 v3, 0, v149\n"
        "v_pk_mul_f32 v[148:149], v[2:3], v[2:3]\n"
        "v_cvt_pk_bf16_f32 v26, v148, v149\n"
        "v_pk_mul_f32 v[150:151], v[150:151], v[12:13] op_sel_hi:[1,0]\n"
        "v_max_f32 v2, 0, v150\n"
        "v_max_f32 v3, 0, v151\n"
        "v_pk_mul_f32 v[150:151], v[2:3], v[2:3]\n"
        "v_cvt_pk_bf16_f32 v27, v150, v151\n"
        "buffer_store_dwordx4 v[24:27], v245, s[64:67], s18 offen offset:0 sc1\n"
        "v_pk_mul_f32 v[152:153], v[152:153], v[12:13] op_sel_hi:[1,0]\n"
        "v_max_f32 v2, 0, v152\n"
        "v_max_f32 v3, 0, v153\n"
        "v_pk_mul_f32 v[152:153], v[2:3], v[2:3]\n"
        "v_cvt_pk_bf16_f32 v28, v152, v153\n"
        "v_pk_mul_f32 v[154:155], v[154:155], v[12:13] op_sel_hi:[1,0]\n"
        "v_max_f32 v2, 0, v154\n"
        "v_max_f32 v3, 0, v155\n"
        "v_pk_mul_f32 v[154:155], v[2:3], v[2:3]\n"
        "v_cvt_pk_bf16_f32 v29, v154, v155\n"
        "v_pk_mul_f32 v[156:157], v[156:157], v[12:13] op_sel_hi:[1,0]\n"
        "v_max_f32 v2, 0, v156\n"
        "v_max_f32 v3, 0, v157\n"
        "v_pk_mul_f32 v[156:157], v[2:3], v[2:3]\n"
        "v_cvt_pk_bf16_f32 v30, v156, v157\n"
        "v_pk_mul_f32 v[158:159], v[158:159], v[12:13] op_sel_hi:[1,0]\n"
        "v_max_f32 v2, 0, v158\n"
        "v_max_f32 v3, 0, v159\n"
        "v_pk_mul_f32 v[158:159], v[2:3], v[2:3]\n"
        "v_cvt_pk_bf16_f32 v31, v158, v159\n"
        "buffer_store_dwordx4 v[28:31], v245, s[64:67], s18 offen offset:1024 sc1\n"
        "s_add_u32 s18, s10, 2\n"
        "s_and_b32 s18, s18, 3\n"
        "s_lshl_b32 s18, s18, 11\n"
        "s_add_u32 s18, s18, s20\n"
        "v_pk_mul_f32 v[160:161], v[160:161], v[12:13] op_sel_hi:[1,0]\n"
        "v_max_f32 v2, 0, v160\n"
        "v_max_f32 v3, 0, v161\n"
        "v_pk_mul_f32 v[160:161], v[2:3], v[2:3]\n"
        "v_cvt_pk_bf16_f32 v32, v160, v161\n"
        "v_pk_mul_f32 v[162:163], v[162:163], v[12:13] op_sel_hi:[1,0]\n"
        "v_max_f32 v2, 0, v162\n"
        "v_max_f32 v3, 0, v163\n"
        "v_pk_mul_f32 v[162:163], v[2:3], v[2:3]\n"
        "v_cvt_pk_bf16_f32 v33, v162, v163\n"
        "v_pk_mul_f32 v[164:165], v[164:165], v[12:13] op_sel_hi:[1,0]\n"
        "v_max_f32 v2, 0, v164\n"
        "v_max_f32 v3, 0, v165\n"
        "v_pk_mul_f32 v[164:165], v[2:3], v[2:3]\n"
        "v_cvt_pk_bf16_f32 v34, v164, v165\n"
        "v_pk_mul_f32 v[166:167], v[166:167], v[12:13] op_sel_hi:[1,0]\n"
        "v_max_f32 v2, 0, v166\n"
        "v_max_f32 v3, 0, v167\n"
        "v_pk_mul_f32 v[166:167], v[2:3], v[2:3]\n"
        "v_cvt_pk_bf16_f32 v35, v166, v167\n"
        "buffer_store_dwordx4 v[32:35], v245, s[64:67], s18 offen offset:0 sc1\n"
        "v_pk_mul_f32 v[168:169], v[168:169], v[12:13] op_sel_hi:[1,0]\n"
        "v_max_f32 v2, 0, v168\n"
        "v_max_f32 v3, 0, v169\n"
        "v_pk_mul_f32 v[168:169], v[2:3], v[2:3]\n"
        "v_cvt_pk_bf16_f32 v36, v168, v169\n"
        "v_pk_mul_f32 v[170:171], v[170:171], v[12:13] op_sel_hi:[1,0]\n"
        "v_max_f32 v2, 0, v170\n"
        "v_max_f32 v3, 0, v171\n"
        "v_pk_mul_f32 v[170:171], v[2:3], v[2:3]\n"
        "v_cvt_pk_bf16_f32 v37, v170, v171\n"
        "v_pk_mul_f32 v[172:173], v[172:173], v[12:13] op_sel_hi:[1,0]\n"
        "v_max_f32 v2, 0, v172\n"
        "v_max_f32 v3, 0, v173\n"
        "v_pk_mul_f32 v[172:173], v[2:3], v[2:3]\n"
        "v_cvt_pk_bf16_f32 v38, v172, v173\n"
        "v_pk_mul_f32 v[174:175], v[174:175], v[12:13] op_sel_hi:[1,0]\n"
        "v_max_f32 v2, 0, v174\n"
        "v_max_f32 v3, 0, v175\n"
        "v_pk_mul_f32 v[174:175], v[2:3], v[2:3]\n"
        "v_cvt_pk_bf16_f32 v39, v174, v175\n"
        "buffer_store_dwordx4 v[36:39], v245, s[64:67], s18 offen offset:1024 sc1\n"
        "s_add_u32 s18, s10, 3\n"
        "s_and_b32 s18, s18, 3\n"
        "s_lshl_b32 s18, s18, 11\n"
        "s_add_u32 s18, s18, s20\n"
        "v_pk_mul_f32 v[176:177], v[176:177], v[12:13] op_sel_hi:[1,0]\n"
        "v_max_f32 v2, 0, v176\n"
        "v_max_f32 v3, 0, v177\n"
        "v_pk_mul_f32 v[176:177], v[2:3], v[2:3]\n"
        "v_cvt_pk_bf16_f32 v40, v176, v177\n"
        "v_pk_mul_f32 v[178:179], v[178:179], v[12:13] op_sel_hi:[1,0]\n"
        "v_max_f32 v2, 0, v178\n"
        "v_max_f32 v3, 0, v179\n"
        "v_pk_mul_f32 v[178:179], v[2:3], v[2:3]\n"
        "v_cvt_pk_bf16_f32 v41, v178, v179\n"
        "v_pk_mul_f32 v[180:181], v[180:181], v[12:13] op_sel_hi:[1,0]\n"
        "v_max_f32 v2, 0, v180\n"
        "v_max_f32 v3, 0, v181\n"
        "v_pk_mul_f32 v[180:181], v[2:3], v[2:3]\n"
        "v_cvt_pk_bf16_f32 v42, v180, v181\n"
        "v_pk_mul_f32 v[182:183], v[182:183], v[12:13] op_sel_hi:[1,0]\n"
        "v_max_f32 v2, 0, v182\n"
        "v_max_f32 v3, 0, v183\n"
        "v_pk_mul_f32 v[182:183], v[2:3], v[2:3]\n"
        "v_cvt_pk_bf16_f32 v43, v182, v183\n"
        "buffer_store_dwordx4 v[40:43], v245, s[64:67], s18 offen offset:0 sc1\n"
        "v_pk_mul_f32 v[184:185], v[184:185], v[12:13] op_sel_hi:[1,0]\n"
        "v_max_f32 v2, 0, v184\n"
        "v_max_f32 v3, 0, v185\n"
        "v_pk_mul_f32 v[184:185], v[2:3], v[2:3]\n"
        "v_cvt_pk_bf16_f32 v44, v184, v185\n"
        "v_pk_mul_f32 v[186:187], v[186:187], v[12:13] op_sel_hi:[1,0]\n"
        "v_max_f32 v2, 0, v186\n"
        "v_max_f32 v3, 0, v187\n"
        "v_pk_mul_f32 v[186:187], v[2:3], v[2:3]\n"
        "v_cvt_pk_bf16_f32 v45, v186, v187\n"
        "v_pk_mul_f32 v[188:189], v[188:189], v[12:13] op_sel_hi:[1,0]\n"
        "v_max_f32 v2, 0, v188\n"
        "v_max_f32 v3, 0, v189\n"
        "v_pk_mul_f32 v[188:189], v[2:3], v[2:3]\n"
        "v_cvt_pk_bf16_f32 v46, v188, v189\n"
        "v_pk_mul_f32 v[190:191], v[190:191], v[12:13] op_sel_hi:[1,0]\n"
        "v_max_f32 v2, 0, v190\n"
        "v_max_f32 v3, 0, v191\n"
        "v_pk_mul_f32 v[190:191], v[2:3], v[2:3]\n"
        "v_cvt_pk_bf16_f32 v47, v190, v191\n"
        "buffer_store_dwordx4 v[44:47], v245, s[64:67], s18 offen offset:1024 sc1\n"
        "s_cmp_lt_u32 s12, 28\n"
        "s_cbranch_scc1 .LG_ffn2\n"
        "s_waitcnt vmcnt(0)\n"
        "s_mov_b64 exec, 1\n"
        "ds_add_rtn_u32 v6, v246, v247 offset:4\n"
        "s_mov_b64 exec, -1\n"
        "s_lshl_b32 s18, s8, 4\n"
        "s_add_u32 s18, s18, s9\n"
        "s_lshl_b32 s18, s18, 7\n"
        "s_add_u32 s18, s18, 36864\n"
        "s_waitcnt lgkmcnt(0)\n"
        "v_readfirstlane_b32 s27, v6\n"
        "s_cmp_lg_u32 s27, 3\n"
        "s_cbranch_scc1 .Lnl_47\n"
        "v_mov_b32 v6, s68\n"
        "v_mov_b32 v7, s69\n"
        "s_mov_b64 exec, 1\n"
        "buffer_store_dwordx2 v[6:7], off, s[64:67], s18 sc1\n"
        "s_mov_b64 exec, -1\n"
        ".Lnl_47:\n"
        ".LG_end0:\n"
        "s_endpgm\n"
        ".LG_ffn2:\n"
        "s_cmp_ge_u32 s12, 28\n"
        "s_cbranch_scc1 .LG_end0\n"
        "v_lshrrev_b32 v1, 5, v0\n"
        "v_lshlrev_b32 v1, 4, v1\n"
        "v_add_u32 v1, s97, v1\n"
        "v_add_u32 v2, s76, v236\n"
        "v_add_u32 v3, s76, v237\n"
        "v_add_u32 v4, 0x10000, v2\n"
        "v_add_u32 v5, 0x10000, v3\n"
        "s_mul_i32 s71, s10, 100352\n"
        "s_cmp_lt_u32 s8, 14\n"
        "s_cbranch_scc1 .LG_hf\n"
        "s_add_u32 m0, s76, 65536\n"
        "s_mul_i32 s80, s77, 14336\n"
        "buffer_load_dwordx4 v232, s[52:55], s80 offen  lds\n"
        "s_add_u32 m0, s76, 66560\n"
        "s_nop 0\n"
        "buffer_load_dwordx4 v233, s[52:55], s80 offen  lds\n"
        "s_add_u32 s77, s77, 1\n"
        "s_cmp_eq_u32 s77, s78\n"
        "s_cselect_b32 s77, s79, s77\n"
        "s_add_u32 m0, s76, 90112\n"
        "s_mul_i32 s80, s77, 14336\n"
        "buffer_load_dwordx4 v232, s[52:55], s80 offen  lds\n"
        "s_add_u32 m0, s76, 91136\n"
        "s_nop 0\n"
        "buffer_load_dwordx4 v233, s[52:55], s80 offen  lds\n"
        "s_add_u32 s77, s77, 1\n"
        "s_cmp_eq_u32 s77, s78\n"
        "s_cselect_b32 s77, s79, s77\n"
        "s_add_u32 m0, s76, 98304\n"
        "s_mul_i32 s80, s77, 14336\n"
        "buffer_load_dwordx4 v232, s[52:55], s80 offen  lds\n"
        "s_add_u32 m0, s76, 99328\n"
        "s_nop 0\n"
        "buffer_load_dwordx4 v233, s[52:55], s80 offen  lds\n"
        "s_add_u32 s77, s77, 1\n"
        "s_cmp_eq_u32 s77, s78\n"
        "s_cselect_b32 s77, s79, s77\n"
        "s_waitcnt vmcnt(4)\n"
        "s_add_u32 m0, s76, 0\n"
        "s_mul_i32 s80, s77, 14336\n"
        "buffer_load_dwordx4 v232, s[52:55], s80 offen  lds\n"
        "s_add_u32 m0, s76, 1024\n"
        "s_nop 0\n"
        "buffer_load_dwordx4 v233, s[52:55], s80 offen  lds\n"
        "s_add_u32 s77, s77, 1\n"
        "s_cmp_eq_u32 s77, s78\n"
        "s_cselect_b32 s77, s79, s77\n"
        "s_waitcnt vmcnt(4)\n"
        "s_add_u32 m0, s76, 8192\n"
        "s_mul_i32 s80, s77, 14336\n"
        "buffer_load_dwordx4 v232, s[52:55], s80 offen  lds\n"
        "s_add_u32 m0, s76, 9216\n"
        "s_nop 0\n"
        "buffer_load_dwordx4 v233, s[52:55], s80 offen  lds\n"
        "s_add_u32 s77, s77, 1\n"
        "s_cmp_eq_u32 s77, s78\n"
        "s_cselect_b32 s77, s79, s77\n"
        "s_waitcnt vmcnt(4)\n"
        "s_add_u32 m0, s76, 16384\n"
        "s_mul_i32 s80, s77, 14336\n"
        "buffer_load_dwordx4 v232, s[52:55], s80 offen  lds\n"
        "s_add_u32 m0, s76, 17408\n"
        "s_nop 0\n"
        "buffer_load_dwordx4 v233, s[52:55], s80 offen  lds\n"
        "s_add_u32 s77, s77, 1\n"
        "s_cmp_eq_u32 s77, s78\n"
        "s_cselect_b32 s77, s79, s77\n"
        "s_waitcnt vmcnt(4)\n"
        "s_add_u32 m0, s76, 24576\n"
        "s_mul_i32 s80, s77, 14336\n"
        "buffer_load_dwordx4 v232, s[52:55], s80 offen  lds\n"
        "s_add_u32 m0, s76, 25600\n"
        "s_nop 0\n"
        "buffer_load_dwordx4 v233, s[52:55], s80 offen  lds\n"
        "s_add_u32 s77, s77, 1\n"
        "s_cmp_eq_u32 s77, s78\n"
        "s_cselect_b32 s77, s79, s77\n"
        "s_waitcnt vmcnt(4)\n"
        "s_add_u32 m0, s76, 32768\n"
        "s_mul_i32 s80, s77, 14336\n"
        "buffer_load_dwordx4 v232, s[52:55], s80 offen  lds\n"
        "s_add_u32 m0, s76, 33792\n"
        "s_nop 0\n"
        "buffer_load_dwordx4 v233, s[52:55], s80 offen  lds\n"
        "s_add_u32 s77, s77, 1\n"
        "s_cmp_eq_u32 s77, s78\n"
        "s_cselect_b32 s77, s79, s77\n"
        "s_waitcnt vmcnt(4)\n"
        "s_add_u32 m0, s76, 40960\n"
        "s_mul_i32 s80, s77, 14336\n"
        "buffer_load_dwordx4 v232, s[52:55], s80 offen  lds\n"
        "s_add_u32 m0, s76, 41984\n"
        "s_nop 0\n"
        "buffer_load_dwordx4 v233, s[52:55], s80 offen  lds\n"
        "s_add_u32 s77, s77, 1\n"
        "s_cmp_eq_u32 s77, s78\n"
        "s_cselect_b32 s77, s79, s77\n"
        "s_waitcnt vmcnt(0)\n"
        "s_branch .LG_nohf\n"
        ".LG_hf:\n"
        "s_waitcnt vmcnt(0)\n"
        "s_mov_b64 exec, 1\n"
        "ds_add_rtn_u32 v6, v246, v247 offset:4\n"
        "s_mov_b64 exec, -1\n"
        "s_lshl_b32 s18, s8, 4\n"
        "s_add_u32 s18, s18, s9\n"
        "s_lshl_b32 s18, s18, 7\n"
        "s_add_u32 s18, s18, 36864\n"
        "s_waitcnt lgkmcnt(0)\n"
        "v_readfirstlane_b32 s27, v6\n"
        "s_cmp_lg_u32 s27, 3\n"
        "s_cbranch_scc1 .LG_nohf\n"
        "v_mov_b32 v6, s68\n"
        "v_mov_b32 v7, s69\n"
        "s_mov_b64 exec, 1\n"
        "buffer_store_dwordx2 v[6:7], off, s[64:67], s18 sc1\n"
        "s_mov_b64 exec, -1\n"
        ".LG_nohf:\n"
        "s_setprio 0\n"
        "buffer_load_dwordx4 v[200:203], v238, s[32:35], 0 offen\n"
        "buffer_load_dwordx4 v[204:207], v238, s[32:35], 0 offen offset:32\n"
        "buffer_load_dwordx4 v[208:211], v1, s[56:59], 0 offen\n"
        "buffer_load_dwordx4 v[212:215], v1, s[56:59], 0 offen offset:32\n"
        "ds_read_b64_tr_b16 v[16:17], v4 offset:0\n"
        "ds_read_b64_tr_b16 v[18:19], v4 offset:512\n"
        "ds_read_b64_tr_b16 v[20:21], v5 offset:0\n"
        "ds_read_b64_tr_b16 v[22:23], v5 offset:512\n"
        "ds_read_b64_tr_b16 v[24:25], v4 offset:24576\n"
        "ds_read_b64_tr_b16 v[26:27], v4 offset:25088\n"
        "ds_read_b64_tr_b16 v[28:29], v5 offset:24576\n"
        "ds_read_b64_tr_b16 v[30:31], v5 offset:25088\n"
        "s_add_u32 m0, s76, 49152\n"
        "s_mul_i32 s80, s77, 14336\n"
        "buffer_load_dwordx4 v232, s[52:55], s80 offen  lds\n"
        "s_add_u32 m0, s76, 50176\n"
        "s_nop 0\n"
        "buffer_load_dwordx4 v233, s[52:55], s80 offen  lds\n"
        "s_add_u32 s77, s77, 1\n"
        "s_cmp_eq_u32 s77, s78\n"
        "s_cselect_b32 s77, s79, s77\n"
        "s_waitcnt lgkmcnt(4)\n"
        "ds_read_b64_tr_b16 v[32:33], v4 offset:32768\n"
        "ds_read_b64_tr_b16 v[34:35], v4 offset:33280\n"
        "ds_read_b64_tr_b16 v[36:37], v5 offset:32768\n"
        "ds_read_b64_tr_b16 v[38:39], v5 offset:33280\n"
        "s_waitcnt lgkmcnt(4)\n"
        "ds_read_b64_tr_b16 v[40:41], v2 offset:0\n"
        "ds_read_b64_tr_b16 v[42:43], v2 offset:512\n"
        "ds_read_b64_tr_b16 v[44:45], v3 offset:0\n"
        "ds_read_b64_tr_b16 v[46:47], v3 offset:512\n"
        "s_waitcnt vmcnt(6)\n"
        "s_add_u32 m0, s76, 57344\n"
        "s_mul_i32 s80, s77, 14336\n"
        "buffer_load_dwordx4 v232, s[52:55], s80 offen  lds\n"
        "s_add_u32 m0, s76, 58368\n"
        "s_nop 0\n"
        "buffer_load_dwordx4 v233, s[52:55], s80 offen  lds\n"
        "s_add_u32 s77, s77, 1\n"
        "s_cmp_eq_u32 s77, s78\n"
        "s_cselect_b32 s77, s79, s77\n"
        "s_waitcnt lgkmcnt(4)\n"
        "ds_read_b64_tr_b16 v[48:49], v2 offset:8192\n"
        "ds_read_b64_tr_b16 v[50:51], v2 offset:8704\n"
        "ds_read_b64_tr_b16 v[52:53], v3 offset:8192\n"
        "ds_read_b64_tr_b16 v[54:55], v3 offset:8704\n"
        "s_waitcnt lgkmcnt(4)\n"
        "ds_read_b64_tr_b16 v[56:57], v2 offset:16384\n"
        "ds_read_b64_tr_b16 v[58:59], v2 offset:16896\n"
        "ds_read_b64_tr_b16 v[60:61], v3 offset:16384\n"
        "ds_read_b64_tr_b16 v[62:63], v3 offset:16896\n"
        "s_waitcnt vmcnt(4)\n"
        "s_add_u32 m0, s76, 73728\n"
        "s_mul_i32 s80, s77, 14336\n"
        "buffer_load_dwordx4 v232, s[52:55], s80 offen  lds\n"
        "s_add_u32 m0, s76, 74752\n"
        "s_nop 0\n"
        "buffer_load_dwordx4 v233, s[52:55], s80 offen  lds\n"
        "s_add_u32 s77, s77, 1\n"
        "s_cmp_eq_u32 s77, s78\n"
        "s_cselect_b32 s77, s79, s77\n"
        "s_waitcnt lgkmcnt(4)\n"
        "ds_read_b64_tr_b16 v[64:65], v2 offset:24576\n"
        "ds_read_b64_tr_b16 v[66:67], v2 offset:25088\n"
        "ds_read_b64_tr_b16 v[68:69], v3 offset:24576\n"
        "ds_read_b64_tr_b16 v[70:71], v3 offset:25088\n"
        "s_waitcnt lgkmcnt(4)\n"
        "ds_read_b64_tr_b16 v[72:73], v2 offset:32768\n"
        "ds_read_b64_tr_b16 v[74:75], v2 offset:33280\n"
        "ds_read_b64_tr_b16 v[76:77], v3 offset:32768\n"
        "ds_read_b64_tr_b16 v[78:79], v3 offset:33280\n"
        "s_add_u32 m0, s76, 81920\n"
        "s_mul_i32 s80, s77, 14336\n"
        "buffer_load_dwordx4 v232, s[52:55], s80 offen  lds\n"
        "s_add_u32 m0, s76, 82944\n"
        "s_nop 0\n"
        "buffer_load_dwordx4 v233, s[52:55], s80 offen  lds\n"
        "s_add_u32 s77, s77, 1\n"
        "s_cmp_eq_u32 s77, s78\n"
        "s_cselect_b32 s77, s79, s77\n"
        "s_setprio 1\n"
        "v_lshlrev_b32 v128, 16, v200\n"
        "v_and_b32 v129, 0xffff0000, v200\n"
        "v_lshlrev_b32 v6, 16, v208\n"
        "v_and_b32 v7, 0xffff0000, v208\n"
        "v_add_f32 v128, v128, v6\n"
        "v_add_f32 v129, v129, v7\n"
        "v_lshlrev_b32 v130, 16, v201\n"
        "v_and_b32 v131, 0xffff0000, v201\n"
        "v_lshlrev_b32 v6, 16, v209\n"
        "v_and_b32 v7, 0xffff0000, v209\n"
        "v_add_f32 v130, v130, v6\n"
        "v_add_f32 v131, v131, v7\n"
        "v_lshlrev_b32 v132, 16, v202\n"
        "v_and_b32 v133, 0xffff0000, v202\n"
        "v_lshlrev_b32 v6, 16, v210\n"
        "v_and_b32 v7, 0xffff0000, v210\n"
        "v_add_f32 v132, v132, v6\n"
        "v_add_f32 v133, v133, v7\n"
        "v_lshlrev_b32 v134, 16, v203\n"
        "v_and_b32 v135, 0xffff0000, v203\n"
        "v_lshlrev_b32 v6, 16, v211\n"
        "v_and_b32 v7, 0xffff0000, v211\n"
        "v_add_f32 v134, v134, v6\n"
        "v_add_f32 v135, v135, v7\n"
        "v_lshlrev_b32 v136, 16, v204\n"
        "v_and_b32 v137, 0xffff0000, v204\n"
        "v_lshlrev_b32 v6, 16, v212\n"
        "v_and_b32 v7, 0xffff0000, v212\n"
        "v_add_f32 v136, v136, v6\n"
        "v_add_f32 v137, v137, v7\n"
        "v_lshlrev_b32 v138, 16, v205\n"
        "v_and_b32 v139, 0xffff0000, v205\n"
        "v_lshlrev_b32 v6, 16, v213\n"
        "v_and_b32 v7, 0xffff0000, v213\n"
        "v_add_f32 v138, v138, v6\n"
        "v_add_f32 v139, v139, v7\n"
        "v_lshlrev_b32 v140, 16, v206\n"
        "v_and_b32 v141, 0xffff0000, v206\n"
        "v_lshlrev_b32 v6, 16, v214\n"
        "v_and_b32 v7, 0xffff0000, v214\n"
        "v_add_f32 v140, v140, v6\n"
        "v_add_f32 v141, v141, v7\n"
        "v_lshlrev_b32 v142, 16, v207\n"
        "v_and_b32 v143, 0xffff0000, v207\n"
        "v_lshlrev_b32 v6, 16, v215\n"
        "v_and_b32 v7, 0xffff0000, v215\n"
        "v_add_f32 v142, v142, v6\n"
        "v_add_f32 v143, v143, v7\n"
        "s_setprio 0\n"
        "s_waitcnt lgkmcnt(4)\n"
        "ds_read_b64_tr_b16 v[80:81], v2 offset:40960\n"
        "ds_read_b64_tr_b16 v[82:83], v2 offset:41472\n"
        "ds_read_b64_tr_b16 v[84:85], v3 offset:40960\n"
        "ds_read_b64_tr_b16 v[86:87], v3 offset:41472\n"
        "s_waitcnt lgkmcnt(4)\n"
        "s_waitcnt vmcnt(6)\n"
        "ds_read_b64_tr_b16 v[88:89], v2 offset:49152\n"
        "ds_read_b64_tr_b16 v[90:91], v2 offset:49664\n"
        "ds_read_b64_tr_b16 v[92:93], v3 offset:49152\n"
        "ds_read_b64_tr_b16 v[94:95], v3 offset:49664\n"
        "s_add_u32 m0, s76, 65536\n"
        "s_mul_i32 s80, s77, 14336\n"
        "buffer_load_dwordx4 v232, s[52:55], s80 offen  lds\n"
        "s_add_u32 m0, s76, 66560\n"
        "s_nop 0\n"
        "buffer_load_dwordx4 v233, s[52:55], s80 offen  lds\n"
        "s_add_u32 s77, s77, 1\n"
        "s_cmp_eq_u32 s77, s78\n"
        "s_cselect_b32 s77, s79, s77\n"
        "s_waitcnt lgkmcnt(4)\n"
        "s_waitcnt vmcnt(6)\n"
        "ds_read_b64_tr_b16 v[96:97], v2 offset:57344\n"
        "ds_read_b64_tr_b16 v[98:99], v2 offset:57856\n"
        "ds_read_b64_tr_b16 v[100:101], v3 offset:57344\n"
        "ds_read_b64_tr_b16 v[102:103], v3 offset:57856\n"
        "s_add_u32 m0, s76, 90112\n"
        "s_mul_i32 s80, s77, 14336\n"
        "buffer_load_dwordx4 v232, s[52:55], s80 offen  lds\n"
        "s_add_u32 m0, s76, 91136\n"
        "s_nop 0\n"
        "buffer_load_dwordx4 v233, s[52:55], s80 offen  lds\n"
        "s_add_u32 s77, s77, 1\n"
        "s_cmp_eq_u32 s77, s78\n"
        "s_cselect_b32 s77, s79, s77\n"
        "s_waitcnt lgkmcnt(4)\n"
        "s_waitcnt vmcnt(6)\n"
        "ds_read_b64_tr_b16 v[104:105], v4 offset:8192\n"
        "ds_read_b64_tr_b16 v[106:107], v4 offset:8704\n"
        "ds_read_b64_tr_b16 v[108:109], v5 offset:8192\n"
        "ds_read_b64_tr_b16 v[110:111], v5 offset:8704\n"
        "s_add_u32 m0, s76, 98304\n"
        "s_mul_i32 s80, s77, 14336\n"
        "buffer_load_dwordx4 v232, s[52:55], s80 offen  lds\n"
        "s_add_u32 m0, s76, 99328\n"
        "s_nop 0\n"
        "buffer_load_dwordx4 v233, s[52:55], s80 offen  lds\n"
        "s_add_u32 s77, s77, 1\n"
        "s_cmp_eq_u32 s77, s78\n"
        "s_cselect_b32 s77, s79, s77\n"
        "s_waitcnt lgkmcnt(4)\n"
        "s_waitcnt vmcnt(6)\n"
        "ds_read_b64_tr_b16 v[112:113], v4 offset:16384\n"
        "ds_read_b64_tr_b16 v[114:115], v4 offset:16896\n"
        "ds_read_b64_tr_b16 v[116:117], v5 offset:16384\n"
        "ds_read_b64_tr_b16 v[118:119], v5 offset:16896\n"
        "s_add_u32 m0, s76, 0\n"
        "s_mul_i32 s80, s77, 14336\n"
        "buffer_load_dwordx4 v232, s[52:55], s80 offen  lds\n"
        "s_add_u32 m0, s76, 1024\n"
        "s_nop 0\n"
        "buffer_load_dwordx4 v233, s[52:55], s80 offen  lds\n"
        "s_add_u32 s77, s77, 1\n"
        "s_cmp_eq_u32 s77, s78\n"
        "s_cselect_b32 s77, s79, s77\n"
        "s_waitcnt lgkmcnt(4)\n"
        "s_waitcnt vmcnt(6)\n"
        "ds_read_b64_tr_b16 v[120:121], v4 offset:0\n"
        "ds_read_b64_tr_b16 v[122:123], v4 offset:512\n"
        "ds_read_b64_tr_b16 v[124:125], v5 offset:0\n"
        "ds_read_b64_tr_b16 v[126:127], v5 offset:512\n"
        "s_add_u32 m0, s76, 8192\n"
        "s_mul_i32 s80, s77, 14336\n"
        "buffer_load_dwordx4 v232, s[52:55], s80 offen  lds\n"
        "s_add_u32 m0, s76, 9216\n"
        "s_nop 0\n"
        "buffer_load_dwordx4 v233, s[52:55], s80 offen  lds\n"
        "s_add_u32 s77, s77, 1\n"
        "s_cmp_eq_u32 s77, s78\n"
        "s_cselect_b32 s77, s79, s77\n"
        "s_waitcnt lgkmcnt(4)\n"
        "s_waitcnt vmcnt(6)\n"
        "ds_read_b64_tr_b16 v[192:193], v4 offset:24576\n"
        "ds_read_b64_tr_b16 v[194:195], v4 offset:25088\n"
        "ds_read_b64_tr_b16 v[196:197], v5 offset:24576\n"
        "ds_read_b64_tr_b16 v[198:199], v5 offset:25088\n"
        "s_add_u32 m0, s76, 16384\n"
        "s_mul_i32 s80, s77, 14336\n"
        "buffer_load_dwordx4 v232, s[52:55], s80 offen  lds\n"
        "s_add_u32 m0, s76, 17408\n"
        "s_nop 0\n"
        "buffer_load_dwordx4 v233, s[52:55], s80 offen  lds\n"
        "s_add_u32 s77, s77, 1\n"
        "s_cmp_eq_u32 s77, s78\n"
        "s_cselect_b32 s77, s79, s77\n"
        "s_waitcnt vmcnt(6)\n"
        "s_add_u32 m0, s76, 24576\n"
        "s_mul_i32 s80, s77, 14336\n"
        "buffer_load_dwordx4 v232, s[52:55], s80 offen  lds\n"
        "s_add_u32 m0, s76, 25600\n"
        "s_nop 0\n"
        "buffer_load_dwordx4 v233, s[52:55], s80 offen  lds\n"
        "s_add_u32 s77, s77, 1\n"
        "s_cmp_eq_u32 s77, s78\n"
        "s_cselect_b32 s77, s79, s77\n"
        "s_waitcnt vmcnt(6)\n"
        "s_add_u32 m0, s76, 32768\n"
        "s_mul_i32 s80, s77, 14336\n"
        "buffer_load_dwordx4 v232, s[52:55], s80 offen  lds\n"
        "s_add_u32 m0, s76, 33792\n"
        "s_nop 0\n"
        "buffer_load_dwordx4 v233, s[52:55], s80 offen  lds\n"
        "s_add_u32 s77, s77, 1\n"
        "s_cmp_eq_u32 s77, s78\n"
        "s_cselect_b32 s77, s79, s77\n"
        "s_waitcnt vmcnt(6)\n"
        "s_add_u32 m0, s76, 40960\n"
        "s_mul_i32 s80, s77, 14336\n"
        "buffer_load_dwordx4 v232, s[52:55], s80 offen  lds\n"
        "s_add_u32 m0, s76, 41984\n"
        "s_nop 0\n"
        "buffer_load_dwordx4 v233, s[52:55], s80 offen  lds\n"
        "s_add_u32 s77, s77, 1\n"
        "s_cmp_eq_u32 s77, s78\n"
        "s_cselect_b32 s77, s79, s77\n"
        "s_waitcnt vmcnt(6)\n"
        "s_add_u32 m0, s76, 49152\n"
        "s_mul_i32 s80, s77, 14336\n"
        "buffer_load_dwordx4 v232, s[52:55], s80 offen  lds\n"
        "s_add_u32 m0, s76, 50176\n"
        "s_nop 0\n"
        "buffer_load_dwordx4 v233, s[52:55], s80 offen  lds\n"
        "s_add_u32 s77, s77, 1\n"
        "s_cmp_eq_u32 s77, s78\n"
        "s_cselect_b32 s77, s79, s77\n"
        "s_waitcnt vmcnt(6)\n"
        "s_add_u32 m0, s76, 57344\n"
        "s_mul_i32 s80, s77, 14336\n"
        "buffer_load_dwordx4 v232, s[52:55], s80 offen  lds\n"
        "s_add_u32 m0, s76, 58368\n"
        "s_nop 0\n"
        "buffer_load_dwordx4 v233, s[52:55], s80 offen  lds\n"
        "s_add_u32 s77, s77, 1\n"
        "s_cmp_eq_u32 s77, s78\n"
        "s_cselect_b32 s77, s79, s77\n"
        "s_waitcnt vmcnt(6)\n"
        "s_add_u32 m0, s76, 73728\n"
        "s_mul_i32 s80, s77, 14336\n"
        "buffer_load_dwordx4 v232, s[52:55], s80 offen  lds\n"
        "s_add_u32 m0, s76, 74752\n"
        "s_nop 0\n"
        "buffer_load_dwordx4 v233, s[52:55], s80 offen  lds\n"
        "s_add_u32 s77, s77, 1\n"
        "s_cmp_eq_u32 s77, s78\n"
        "s_cselect_b32 s77, s79, s77\n"
        "s_waitcnt vmcnt(6)\n"
        "s_add_u32 m0, s76, 81920\n"
        "s_mul_i32 s80, s77, 14336\n"
        "buffer_load_dwordx4 v232, s[52:55], s80 offen  lds\n"
        "s_add_u32 m0, s76, 82944\n"
        "s_nop 0\n"
        "buffer_load_dwordx4 v233, s[52:55], s80 offen  lds\n"
        "s_add_u32 s77, s77, 1\n"
        "s_cmp_eq_u32 s77, s78\n"
        "s_cselect_b32 s77, s79, s77\n"
        "s_waitcnt vmcnt(6)\n"
        "s_waitcnt lgkmcnt(4)\n"
        "s_add_u32 m0, s76, 65536\n"
        "s_mul_i32 s80, s77, 14336\n"
        "buffer_load_dwordx4 v232, s[52:55], s80 offen  lds\n"
        "s_add_u32 m0, s76, 66560\n"
        "s_nop 0\n"
        "buffer_load_dwordx4 v233, s[52:55], s80 offen  lds\n"
        "s_add_u32 s77, s77, 1\n"
        "s_cmp_eq_u32 s77, s78\n"
        "s_cselect_b32 s77, s79, s77\n"
        "s_waitcnt vmcnt(6)\n"
        "s_waitcnt lgkmcnt(0)\n"
        "s_add_u32 m0, s76, 90112\n"
        "s_mul_i32 s80, s77, 14336\n"
        "buffer_load_dwordx4 v232, s[52:55], s80 offen  lds\n"
        "s_add_u32 m0, s76, 91136\n"
        "s_nop 0\n"
        "buffer_load_dwordx4 v233, s[52:55], s80 offen  lds\n"
        "s_add_u32 s77, s77, 1\n"
        "s_cmp_eq_u32 s77, s78\n"
        "s_cselect_b32 s77, s79, s77\n"
        "s_mov_b32 s26, 0\n"
        ".Lpoll_48:\n"
        "buffer_load_dwordx2 v[6:7], v239, s[64:67], 0 offen sc1\n"
        "s_add_u32 s26, s26, 1\n"
        "s_waitcnt vmcnt(0)\n"
        "s_setprio 1\n"
        "v_cmp_ne_u64 vcc, s[68:69], v[6:7]\n"
        "s_setprio 0\n"
        "s_cbranch_vccz .Lpdone_49\n"
        "s_sleep 4\n"
        "s_cmp_lt_u32 s26, 50000\n"
        "s_cbranch_scc1 .Lpoll_48\n"
        ".Lpdone_49:\n"
        "s_lshl_b32 s81, s96, 10\n"
        "buffer_load_dwordx4 v[200:203], v234, s[64:67], s81 offen sc1\n"
        "buffer_load_dwordx4 v[204:207], v235, s[64:67], s81 offen sc1\n"
        "s_add_u32 s96, s96, 1\n"
        "s_cmp_eq_u32 s96, s78\n"
        "s_cselect_b32 s96, s79, s96\n"
        "s_lshl_b32 s81, s96, 10\n"
        "buffer_load_dwordx4 v[208:211], v234, s[64:67], s81 offen sc1\n"
        "buffer_load_dwordx4 v[212:215], v235, s[64:67], s81 offen sc1\n"
        "s_add_u32 s96, s96, 1\n"
        "s_cmp_eq_u32 s96, s78\n"
        "s_cselect_b32 s96, s79, s96\n"
        "s_lshl_b32 s81, s96, 10\n"
        "buffer_load_dwordx4 v[216:219], v234, s[64:67], s81 offen sc1\n"
        "buffer_load_dwordx4 v[220:223], v235, s[64:67], s81 offen sc1\n"
        "s_add_u32 s96, s96, 1\n"
        "s_cmp_eq_u32 s96, s78\n"
        "s_cselect_b32 s96, s79, s96\n"
        "s_waitcnt vmcnt(4)\n"
        "v_mfma_f32_32x32x16_bf16 v[128:143], v[16:19], v[200:203], v[128:143]\n"
        "v_mfma_f32_32x32x16_bf16 v[144:159], v[16:19], v[204:207], 0\n"
        "v_mfma_f32_32x32x16_bf16 v[160:175], v[20:23], v[200:203], 0\n"
        "v_mfma_f32_32x32x16_bf16 v[176:191], v[20:23], v[204:207], 0\n"
        "ds_read_b64_tr_b16 v[16:17], v4 offset:32768\n"
        "ds_read_b64_tr_b16 v[18:19], v4 offset:33280\n"
        "ds_read_b64_tr_b16 v[20:21], v5 offset:32768\n"
        "ds_read_b64_tr_b16 v[22:23], v5 offset:33280\n"
        "s_lshl_b32 s81, s96, 10\n"
        "buffer_load_dwordx4 v[224:227], v234, s[64:67], s81 offen sc1\n"
        "buffer_load_dwordx4 v[228:231], v235, s[64:67], s81 offen sc1\n"
        "s_add_u32 s96, s96, 1\n"
        "s_cmp_eq_u32 s96, s78\n"
        "s_cselect_b32 s96, s79, s96\n"
        "s_waitcnt vmcnt(4)\n"
        "v_mfma_f32_32x32x16_bf16 v[128:143], v[24:27], v[208:211], v[128:143]\n"
        "v_mfma_f32_32x32x16_bf16 v[144:159], v[24:27], v[212:215], v[144:159]\n"
        "v_mfma_f32_32x32x16_bf16 v[160:175], v[28:31], v[208:211], v[160:175]\n"
        "v_mfma_f32_32x32x16_bf16 v[176:191], v[28:31], v[212:215], v[176:191]\n"
        "ds_read_b64_tr_b16 v[24:25], v2 offset:0\n"
        "ds_read_b64_tr_b16 v[26:27], v2 offset:512\n"
        "ds_read_b64_tr_b16 v[28:29], v3 offset:0\n"
        "ds_read_b64_tr_b16 v[30:31], v3 offset:512\n"
        "s_lshl_b32 s81, s96, 10\n"
        "buffer_load_dwordx4 v[200:203], v234, s[64:67], s81 offen sc1\n"
        "buffer_load_dwordx4 v[204:207], v235, s[64:67], s81 offen sc1\n"
        "s_add_u32 s96, s96, 1\n"
        "s_cmp_eq_u32 s96, s78\n"
        "s_cselect_b32 s96, s79, s96\n"
        "s_waitcnt vmcnt(4)\n"
        "v_mfma_f32_32x32x16_bf16 v[128:143], v[32:35], v[216:219], v[128:143]\n"
        "v_mfma_f32_32x32x16_bf16 v[144:159], v[32:35], v[220:223], v[144:159]\n"
        "s_waitcnt lgkmcnt(4)\n"
        "s_add_u32 m0, s76, 98304\n"
        "s_nop 0\n"
        "buffer_load_dwordx4 v232, s[40:43], s71 offen  lds\n"
        "s_add_u32 m0, s76, 99328\n"
        "s_nop 0\n"
        "buffer_load_dwordx4 v233, s[40:43], s71 offen  lds\n"
        "s_add_u32 s71, s71, 14336\n"
        "v_mfma_f32_32x32x16_bf16 v[160:175], v[36:39], v[216:219], v[160:175]\n"
        "v_mfma_f32_32x32x16_bf16 v[176:191], v[36:39], v[220:223], v[176:191]\n"
        "ds_read_b64_tr_b16 v[32:33], v2 offset:8192\n"
        "ds_read_b64_tr_b16 v[34:35], v2 offset:8704\n"
        "ds_read_b64_tr_b16 v[36:37], v3 offset:8192\n"
        "ds_read_b64_tr_b16 v[38:39], v3 offset:8704\n"
        "s_lshl_b32 s81, s96, 10\n"
        "buffer_load_dwordx4 v[208:211], v234, s[64:67], s81 offen sc1\n"
        "buffer_load_dwordx4 v[212:215], v235, s[64:67], s81 offen sc1\n"
        "s_add_u32 s96, s96, 1\n"
        "s_cmp_eq_u32 s96, s78\n"
        "s_cselect_b32 s96, s79, s96\n"
        "s_waitcnt vmcnt(6)\n"
        "v_mfma_f32_32x32x16_bf16 v[128:143], v[40:43], v[224:227], v[128:143]\n"
        "v_mfma_f32_32x32x16_bf16 v[144:159], v[40:43], v[228:231], v[144:159]\n"
        "s_waitcnt lgkmcnt(4)\n"
        "s_add_u32 m0, s76, 0\n"
        "s_nop 0\n"
        "buffer_load_dwordx4 v232, s[40:43], s71 offen  lds\n"
        "s_add_u32 m0, s76, 1024\n"
        "s_nop 0\n"
        "buffer_load_dwordx4 v233, s[40:43], s71 offen  lds\n"
        "s_add_u32 s71, s71, 14336\n"
        "v_mfma_f32_32x32x16_bf16 v[160:175], v[44:47], v[224:227], v[160:175]\n"
        "v_mfma_f32_32x32x16_bf16 v[176:191], v[44:47], v[228:231], v[176:191]\n"
        "ds_read_b64_tr_b16 v[40:41], v2 offset:16384\n"
        "ds_read_b64_tr_b16 v[42:43], v2 offset:16896\n"
        "ds_read_b64_tr_b16 v[44:45], v3 offset:16384\n"
        "ds_read_b64_tr_b16 v[46:47], v3 offset:16896\n"
        "s_lshl_b32 s81, s96, 10\n"
        "buffer_load_dwordx4 v[216:219], v234, s[64:67], s81 offen sc1\n"
        "buffer_load_dwordx4 v[220:223], v235, s[64:67], s81 offen sc1\n"
        "s_add_u32 s96, s96, 1\n"
        "s_cmp_eq_u32 s96, s78\n"
        "s_cselect_b32 s96, s79, s96\n"
        "s_waitcnt vmcnt(8)\n"
        "v_mfma_f32_32x32x16_bf16 v[128:143], v[48:51], v[200:203], v[128:143]\n"
        "v_mfma_f32_32x32x16_bf16 v[144:159], v[48:51], v[204:207], v[144:159]\n"
        "s_waitcnt lgkmcnt(4)\n"
        "s_add_u32 m0, s76, 8192\n"
        "s_nop 0\n"
        "buffer_load_dwordx4 v232, s[40:43], s71 offen  lds\n"
        "s_add_u32 m0, s76, 9216\n"
        "s_nop 0\n"
        "buffer_load_dwordx4 v233, s[40:43], s71 offen  lds\n"
        "s_add_u32 s71, s71, 14336\n"
        "v_mfma_f32_32x32x16_bf16 v[160:175], v[52:55], v[200:203], v[160:175]\n"
        "v_mfma_f32_32x32x16_bf16 v[176:191], v[52:55], v[204:207], v[176:191]\n"
        "ds_read_b64_tr_b16 v[48:49], v2 offset:24576\n"
        "ds_read_b64_tr_b16 v[50:51], v2 offset:25088\n"
        "ds_read_b64_tr_b16 v[52:53], v3 offset:24576\n"
        "ds_read_b64_tr_b16 v[54:55], v3 offset:25088\n"
        "s_lshl_b32 s81, s96, 10\n"
        "buffer_load_dwordx4 v[224:227], v234, s[64:67], s81 offen sc1\n"
        "buffer_load_dwordx4 v[228:231], v235, s[64:67], s81 offen sc1\n"
        "s_add_u32 s96, s96, 1\n"
        "s_cmp_eq_u32 s96, s78\n"
        "s_cselect_b32 s96, s79, s96\n"
        "s_waitcnt vmcnt(8)\n"
        "v_mfma_f32_32x32x16_bf16 v[128:143], v[56:59], v[208:211], v[128:143]\n"
        "v_mfma_f32_32x32x16_bf16 v[144:159], v[56:59], v[212:215], v[144:159]\n"
        "s_waitcnt lgkmcnt(4)\n"
        "s_add_u32 m0, s76, 16384\n"
        "s_nop 0\n"
        "buffer_load_dwordx4 v232, s[40:43], s71 offen  lds\n"
        "s_add_u32 m0, s76, 17408\n"
        "s_nop 0\n"
        "buffer_load_dwordx4 v233, s[40:43], s71 offen  lds\n"
        "s_add_u32 s71, s71, 14336\n"
        "v_mfma_f32_32x32x16_bf16 v[160:175], v[60:63], v[208:211], v[160:175]\n"
        "v_mfma_f32_32x32x16_bf16 v[176:191], v[60:63], v[212:215], v[176:191]\n"
        "ds_read_b64_tr_b16 v[56:57], v2 offset:32768\n"
        "ds_read_b64_tr_b16 v[58:59], v2 offset:33280\n"
        "ds_read_b64_tr_b16 v[60:61], v3 offset:32768\n"
        "ds_read_b64_tr_b16 v[62:63], v3 offset:33280\n"
        "s_lshl_b32 s81, s96, 10\n"
        "buffer_load_dwordx4 v[200:203], v234, s[64:67], s81 offen sc1\n"
        "buffer_load_dwordx4 v[204:207], v235, s[64:67], s81 offen sc1\n"
        "s_add_u32 s96, s96, 1\n"
        "s_cmp_eq_u32 s96, s78\n"
        "s_cselect_b32 s96, s79, s96\n"
        "s_waitcnt vmcnt(8)\n"
        "v_mfma_f32_32x32x16_bf16 v[128:143], v[64:67], v[216:219], v[128:143]\n"
        "v_mfma_f32_32x32x16_bf16 v[144:159], v[64:67], v[220:223], v[144:159]\n"
        "s_waitcnt lgkmcnt(4)\n"
        "s_add_u32 m0, s76, 24576\n"
        "s_nop 0\n"
        "buffer_load_dwordx4 v232, s[40:43], s71 offen  lds\n"
        "s_add_u32 m0, s76, 25600\n"
        "s_nop 0\n"
        "buffer_load_dwordx4 v233, s[40:43], s71 offen  lds\n"
        "s_add_u32 s71, s71, 14336\n"
        "v_mfma_f32_32x32x16_bf16 v[160:175], v[68:71], v[216:219], v[160:175]\n"
        "v_mfma_f32_32x32x16_bf16 v[176:191], v[68:71], v[220:223], v[176:191]\n"
        "ds_read_b64_tr_b16 v[64:65], v2 offset:40960\n"
        "ds_read_b64_tr_b16 v[66:67], v2 offset:41472\n"
        "ds_read_b64_tr_b16 v[68:69], v3 offset:40960\n"
        "ds_read_b64_tr_b16 v[70:71], v3 offset:41472\n"
        "s_lshl_b32 s81, s96, 10\n"
        "buffer_load_dwordx4 v[208:211], v234, s[64:67], s81 offen sc1\n"
        "buffer_load_dwordx4 v[212:215], v235, s[64:67], s81 offen sc1\n"
        "s_add_u32 s96, s96, 1\n"
        "s_cmp_eq_u32 s96, s78\n"
        "s_cselect_b32 s96, s79, s96\n"
        "s_waitcnt vmcnt(8)\n"
        "v_mfma_f32_32x32x16_bf16 v[128:143], v[72:75], v[224:227], v[128:143]\n"
        "v_mfma_f32_32x32x16_bf16 v[144:159], v[72:75], v[228:231], v[144:159]\n"
        "s_waitcnt lgkmcnt(4)\n"
        "s_add_u32 m0, s76, 32768\n"
        "s_nop 0\n"
        "buffer_load_dwordx4 v232, s[40:43], s71 offen  lds\n"
        "s_add_u32 m0, s76, 33792\n"
        "s_nop 0\n"
        "buffer_load_dwordx4 v233, s[40:43], s71 offen  lds\n"
        "s_add_u32 s71, s71, 14336\n"
        "v_mfma_f32_32x32x16_bf16 v[160:175], v[76:79], v[224:227], v[160:175]\n"
        "v_mfma_f32_32x32x16_bf16 v[176:191], v[76:79], v[228:231], v[176:191]\n"
        "ds_read_b64_tr_b16 v[72:73], v2 offset:49152\n"
        "ds_read_b64_tr_b16 v[74:75], v2 offset:49664\n"
        "ds_read_b64_tr_b16 v[76:77], v3 offset:49152\n"
        "ds_read_b64_tr_b16 v[78:79], v3 offset:49664\n"
        "s_lshl_b32 s81, s96, 10\n"
        "buffer_load_dwordx4 v[216:219], v234, s[64:67], s81 offen sc1\n"
        "buffer_load_dwordx4 v[220:223], v235, s[64:67], s81 offen sc1\n"
        "s_add_u32 s96, s96, 1\n"
        "s_cmp_eq_u32 s96, s78\n"
        "s_cselect_b32 s96, s79, s96\n"
        "s_waitcnt vmcnt(8)\n"
        "v_mfma_f32_32x32x16_bf16 v[128:143], v[80:83], v[200:203], v[128:143]\n"
        "v_mfma_f32_32x32x16_bf16 v[144:159], v[80:83], v[204:207], v[144:159]\n"
        "s_waitcnt lgkmcnt(4)\n"
        "s_add_u32 m0, s76, 40960\n"
        "s_nop 0\n"
        "buffer_load_dwordx4 v232, s[40:43], s71 offen  lds\n"
        "s_add_u32 m0, s76, 41984\n"
        "s_nop 0\n"
        "buffer_load_dwordx4 v233, s[40:43], s71 offen  lds\n"
        "s_add_u32 s71, s71, 14336\n"
        "v_mfma_f32_32x32x16_bf16 v[160:175], v[84:87], v[200:203], v[160:175]\n"
        "v_mfma_f32_32x32x16_bf16 v[176:191], v[84:87], v[204:207], v[176:191]\n"
        "ds_read_b64_tr_b16 v[80:81], v2 offset:57344\n"
        "ds_read_b64_tr_b16 v[82:83], v2 offset:57856\n"
        "ds_read_b64_tr_b16 v[84:85], v3 offset:57344\n"
        "ds_read_b64_tr_b16 v[86:87], v3 offset:57856\n"
        "s_lshl_b32 s81, s96, 10\n"
        "buffer_load_dwordx4 v[224:227], v234, s[64:67], s81 offen sc1\n"
        "buffer_load_dwordx4 v[228:231], v235, s[64:67], s81 offen sc1\n"
        "s_add_u32 s96, s96, 1\n"
        "s_cmp_eq_u32 s96, s78\n"
        "s_cselect_b32 s96, s79, s96\n"
        "s_waitcnt vmcnt(8)\n"
        "v_mfma_f32_32x32x16_bf16 v[128:143], v[88:91], v[208:211], v[128:143]\n"
        "v_mfma_f32_32x32x16_bf16 v[144:159], v[88:91], v[212:215], v[144:159]\n"
        "v_mfma_f32_32x32x16_bf16 v[160:175], v[92:95], v[208:211], v[160:175]\n"
        "v_mfma_f32_32x32x16_bf16 v[176:191], v[92:95], v[212:215], v[176:191]\n"
        "s_waitcnt lgkmcnt(4)\n"
        "ds_read_b64_tr_b16 v[88:89], v4 offset:8192\n"
        "ds_read_b64_tr_b16 v[90:91], v4 offset:8704\n"
        "ds_read_b64_tr_b16 v[92:93], v5 offset:8192\n"
        "ds_read_b64_tr_b16 v[94:95], v5 offset:8704\n"
        "s_lshl_b32 s81, s96, 10\n"
        "buffer_load_dwordx4 v[200:203], v234, s[64:67], s81 offen sc1\n"
        "buffer_load_dwordx4 v[204:207], v235, s[64:67], s81 offen sc1\n"
        "s_add_u32 s96, s96, 1\n"
        "s_cmp_eq_u32 s96, s78\n"
        "s_cselect_b32 s96, s79, s96\n"
        "s_waitcnt vmcnt(6)\n"
        "v_mfma_f32_32x32x16_bf16 v[128:143], v[96:99], v[216:219], v[128:143]\n"
        "v_mfma_f32_32x32x16_bf16 v[144:159], v[96:99], v[220:223], v[144:159]\n"
        "v_mfma_f32_32x32x16_bf16 v[160:175], v[100:103], v[216:219], v[160:175]\n"
        "v_mfma_f32_32x32x16_bf16 v[176:191], v[100:103], v[220:223], v[176:191]\n"
        "s_waitcnt lgkmcnt(4)\n"
        "ds_read_b64_tr_b16 v[96:97], v4 offset:16384\n"
        "ds_read_b64_tr_b16 v[98:99], v4 offset:16896\n"
        "ds_read_b64_tr_b16 v[100:101], v5 offset:16384\n"
        "ds_read_b64_tr_b16 v[102:103], v5 offset:16896\n"
        "s_lshl_b32 s81, s96, 10\n"
        "buffer_load_dwordx4 v[208:211], v234, s[64:67], s81 offen sc1\n"
        "buffer_load_dwordx4 v[212:215], v235, s[64:67], s81 offen sc1\n"
        "s_add_u32 s96, s96, 1\n"
        "s_cmp_eq_u32 s96, s78\n"
        "s_cselect_b32 s96, s79, s96\n"
        "s_waitcnt vmcnt(4)\n"
        "v_mfma_f32_32x32x16_bf16 v[128:143], v[104:107], v[224:227], v[128:143]\n"
        "v_mfma_f32_32x32x16_bf16 v[144:159], v[104:107], v[228:231], v[144:159]\n"
        "v_mfma_f32_32x32x16_bf16 v[160:175], v[108:111], v[224:227], v[160:175]\n"
        "v_mfma_f32_32x32x16_bf16 v[176:191], v[108:111], v[228:231], v[176:191]\n"
        "s_waitcnt lgkmcnt(4)\n"
        "ds_read_b64_tr_b16 v[104:105], v4 offset:0\n"
        "ds_read_b64_tr_b16 v[106:107], v4 offset:512\n"
        "ds_read_b64_tr_b16 v[108:109], v5 offset:0\n"
        "ds_read_b64_tr_b16 v[110:111], v5 offset:512\n"
        "s_lshl_b32 s81, s96, 10\n"
        "buffer_load_dwordx4 v[216:219], v234, s[64:67], s81 offen sc1\n"
        "buffer_load_dwordx4 v[220:223], v235, s[64:67], s81 offen sc1\n"
        "s_add_u32 s96, s96, 1\n"
        "s_cmp_eq_u32 s96, s78\n"
        "s_cselect_b32 s96, s79, s96\n"
        "s_waitcnt vmcnt(4)\n"
        "v_mfma_f32_32x32x16_bf16 v[128:143], v[112:115], v[200:203], v[128:143]\n"
        "v_mfma_f32_32x32x16_bf16 v[144:159], v[112:115], v[204:207], v[144:159]\n"
        "v_mfma_f32_32x32x16_bf16 v[160:175], v[116:119], v[200:203], v[160:175]\n"
        "v_mfma_f32_32x32x16_bf16 v[176:191], v[116:119], v[204:207], v[176:191]\n"
        "s_waitcnt lgkmcnt(4)\n"
        "ds_read_b64_tr_b16 v[112:113], v4 offset:24576\n"
        "ds_read_b64_tr_b16 v[114:115], v4 offset:25088\n"
        "ds_read_b64_tr_b16 v[116:117], v5 offset:24576\n"
        "ds_read_b64_tr_b16 v[118:119], v5 offset:25088\n"
        "s_lshl_b32 s81, s96, 10\n"
        "buffer_load_dwordx4 v[224:227], v234, s[64:67], s81 offen sc1\n"
        "buffer_load_dwordx4 v[228:231], v235, s[64:67], s81 offen sc1\n"
        "s_add_u32 s96, s96, 1\n"
        "s_cmp_eq_u32 s96, s78\n"
        "s_cselect_b32 s96, s79, s96\n"
        "s_waitcnt vmcnt(4)\n"
        "v_mfma_f32_32x32x16_bf16 v[128:143], v[120:123], v[208:211], v[128:143]\n"
        "v_mfma_f32_32x32x16_bf16 v[144:159], v[120:123], v[212:215], v[144:159]\n"
        "v_mfma_f32_32x32x16_bf16 v[160:175], v[124:127], v[208:211], v[160:175]\n"
        "v_mfma_f32_32x32x16_bf16 v[176:191], v[124:127], v[212:215], v[176:191]\n"
        "s_lshl_b32 s81, s96, 10\n"
        "buffer_load_dwordx4 v[200:203], v234, s[64:67], s81 offen sc1\n"
        "buffer_load_dwordx4 v[204:207], v235, s[64:67], s81 offen sc1\n"
        "s_add_u32 s96, s96, 1\n"
        "s_cmp_eq_u32 s96, s78\n"
        "s_cselect_b32 s96, s79, s96\n"
        "s_waitcnt vmcnt(4)\n"
        "v_mfma_f32_32x32x16_bf16 v[128:143], v[192:195], v[216:219], v[128:143]\n"
        "v_mfma_f32_32x32x16_bf16 v[144:159], v[192:195], v[220:223], v[144:159]\n"
        "v_mfma_f32_32x32x16_bf16 v[160:175], v[196:199], v[216:219], v[160:175]\n"
        "v_mfma_f32_32x32x16_bf16 v[176:191], v[196:199], v[220:223], v[176:191]\n"
        "s_lshl_b32 s81, s96, 10\n"
        "buffer_load_dwordx4 v[208:211], v234, s[64:67], s81 offen sc1\n"
        "buffer_load_dwordx4 v[212:215], v235, s[64:67], s81 offen sc1\n"
        "s_add_u32 s96, s96, 1\n"
        "s_cmp_eq_u32 s96, s78\n"
        "s_cselect_b32 s96, s79, s96\n"
        "s_waitcnt vmcnt(4)\n"
        "v_mfma_f32_32x32x16_bf16 v[128:143], v[16:19], v[224:227], v[128:143]\n"
        "v_mfma_f32_32x32x16_bf16 v[144:159], v[16:19], v[228:231], v[144:159]\n"
        "v_mfma_f32_32x32x16_bf16 v[160:175], v[20:23], v[224:227], v[160:175]\n"
        "v_mfma_f32_32x32x16_bf16 v[176:191], v[20:23], v[228:231], v[176:191]\n"
        "s_lshl_b32 s81, s96, 10\n"
        "buffer_load_dwordx4 v[216:219], v234, s[64:67], s81 offen sc1\n"
        "buffer_load_dwordx4 v[220:223], v235, s[64:67], s81 offen sc1\n"
        "s_add_u32 s96, s96, 1\n"
        "s_cmp_eq_u32 s96, s78\n"
        "s_cselect_b32 s96, s79, s96\n"
        "s_waitcnt vmcnt(4)\n"
        "v_mfma_f32_32x32x16_bf16 v[128:143], v[24:27], v[200:203], v[128:143]\n"
        "v_mfma_f32_32x32x16_bf16 v[144:159], v[24:27], v[204:207], v[144:159]\n"
        "v_mfma_f32_32x32x16_bf16 v[160:175], v[28:31], v[200:203], v[160:175]\n"
        "v_mfma_f32_32x32x16_bf16 v[176:191], v[28:31], v[204:207], v[176:191]\n"
        "s_lshl_b32 s81, s96, 10\n"
        "buffer_load_dwordx4 v[224:227], v234, s[64:67], s81 offen sc1\n"
        "buffer_load_dwordx4 v[228:231], v235, s[64:67], s81 offen sc1\n"
        "s_add_u32 s96, s96, 1\n"
        "s_cmp_eq_u32 s96, s78\n"
        "s_cselect_b32 s96, s79, s96\n"
        "s_waitcnt vmcnt(4)\n"
        "v_mfma_f32_32x32x16_bf16 v[128:143], v[32:35], v[208:211], v[128:143]\n"
        "v_mfma_f32_32x32x16_bf16 v[144:159], v[32:35], v[212:215], v[144:159]\n"
        "v_mfma_f32_32x32x16_bf16 v[160:175], v[36:39], v[208:211], v[160:175]\n"
        "v_mfma_f32_32x32x16_bf16 v[176:191], v[36:39], v[212:215], v[176:191]\n"
        "s_lshl_b32 s81, s96, 10\n"
        "buffer_load_dwordx4 v[200:203], v234, s[64:67], s81 offen sc1\n"
        "buffer_load_dwordx4 v[204:207], v235, s[64:67], s81 offen sc1\n"
        "s_add_u32 s96, s96, 1\n"
        "s_cmp_eq_u32 s96, s78\n"
        "s_cselect_b32 s96, s79, s96\n"
        "s_waitcnt vmcnt(4)\n"
        "v_mfma_f32_32x32x16_bf16 v[128:143], v[40:43], v[216:219], v[128:143]\n"
        "v_mfma_f32_32x32x16_bf16 v[144:159], v[40:43], v[220:223], v[144:159]\n"
        "v_mfma_f32_32x32x16_bf16 v[160:175], v[44:47], v[216:219], v[160:175]\n"
        "v_mfma_f32_32x32x16_bf16 v[176:191], v[44:47], v[220:223], v[176:191]\n"
        "s_lshl_b32 s81, s96, 10\n"
        "buffer_load_dwordx4 v[208:211], v234, s[64:67], s81 offen sc1\n"
        "buffer_load_dwordx4 v[212:215], v235, s[64:67], s81 offen sc1\n"
        "s_add_u32 s96, s96, 1\n"
        "s_cmp_eq_u32 s96, s78\n"
        "s_cselect_b32 s96, s79, s96\n"
        "s_waitcnt vmcnt(4)\n"
        "v_mfma_f32_32x32x16_bf16 v[128:143], v[48:51], v[224:227], v[128:143]\n"
        "v_mfma_f32_32x32x16_bf16 v[144:159], v[48:51], v[228:231], v[144:159]\n"
        "v_mfma_f32_32x32x16_bf16 v[160:175], v[52:55], v[224:227], v[160:175]\n"
        "v_mfma_f32_32x32x16_bf16 v[176:191], v[52:55], v[228:231], v[176:191]\n"
        "s_lshl_b32 s81, s96, 10\n"
        "buffer_load_dwordx4 v[216:219], v234, s[64:67], s81 offen sc1\n"
        "buffer_load_dwordx4 v[220:223], v235, s[64:67], s81 offen sc1\n"
        "s_add_u32 s96, s96, 1\n"
        "s_cmp_eq_u32 s96, s78\n"
        "s_cselect_b32 s96, s79, s96\n"
        "s_waitcnt vmcnt(4)\n"
        "v_mfma_f32_32x32x16_bf16 v[128:143], v[56:59], v[200:203], v[128:143]\n"
        "v_mfma_f32_32x32x16_bf16 v[144:159], v[56:59], v[204:207], v[144:159]\n"
        "v_mfma_f32_32x32x16_bf16 v[160:175], v[60:63], v[200:203], v[160:175]\n"
        "v_mfma_f32_32x32x16_bf16 v[176:191], v[60:63], v[204:207], v[176:191]\n"
        "s_lshl_b32 s81, s96, 10\n"
        "buffer_load_dwordx4 v[224:227], v234, s[64:67], s81 offen sc1\n"
        "buffer_load_dwordx4 v[228:231], v235, s[64:67], s81 offen sc1\n"
        "s_add_u32 s96, s96, 1\n"
        "s_cmp_eq_u32 s96, s78\n"
        "s_cselect_b32 s96, s79, s96\n"
        "s_waitcnt vmcnt(4)\n"
        "v_mfma_f32_32x32x16_bf16 v[128:143], v[64:67], v[208:211], v[128:143]\n"
        "v_mfma_f32_32x32x16_bf16 v[144:159], v[64:67], v[212:215], v[144:159]\n"
        "v_mfma_f32_32x32x16_bf16 v[160:175], v[68:71], v[208:211], v[160:175]\n"
        "v_mfma_f32_32x32x16_bf16 v[176:191], v[68:71], v[212:215], v[176:191]\n"
        "s_lshl_b32 s81, s96, 10\n"
        "buffer_load_dwordx4 v[200:203], v234, s[64:67], s81 offen sc1\n"
        "buffer_load_dwordx4 v[204:207], v235, s[64:67], s81 offen sc1\n"
        "s_add_u32 s96, s96, 1\n"
        "s_cmp_eq_u32 s96, s78\n"
        "s_cselect_b32 s96, s79, s96\n"
        "s_lshl_b32 s81, s96, 10\n"
        "buffer_load_dwordx4 v[208:211], v234, s[64:67], s81 offen sc1\n"
        "buffer_load_dwordx4 v[212:215], v235, s[64:67], s81 offen sc1\n"
        "s_add_u32 s96, s96, 1\n"
        "s_cmp_eq_u32 s96, s78\n"
        "s_cselect_b32 s96, s79, s96\n"
        "s_setprio 1\n"
        "v_mov_b32 v1, 159744\n"
        "s_setprio 0\n"
        "s_mov_b32 s26, 0\n"
        ".Lhold_50:\n"
        "ds_read_b32 v6, v1\n"
        "s_waitcnt lgkmcnt(0)\n"
        "s_setprio 1\n"
        "v_readfirstlane_b32 s27, v6\n"
        "s_setprio 0\n"
        "s_add_u32 s26, s26, 1\n"
        "s_and_b32 s27, s27, 0xffff\n"
        "s_cmp_ge_u32 s27, 5\n"
        "s_cbranch_scc1 .Lhdone_51\n"
        "s_sleep 3\n"
        "s_cmp_lt_u32 s26, 100000\n"
        "s_cbranch_scc1 .Lhold_50\n"
        ".Lhdone_51:\n"
        "s_waitcnt vmcnt(6)\n"
        "v_mfma_f32_32x32x16_bf16 v[128:143], v[72:75], v[216:219], v[128:143]\n"
        "v_mfma_f32_32x32x16_bf16 v[144:159], v[72:75], v[220:223], v[144:159]\n"
        "v_mfma_f32_32x32x16_bf16 v[160:175], v[76:79], v[216:219], v[160:175]\n"
        "v_mfma_f32_32x32x16_bf16 v[176:191], v[76:79], v[220:223], v[176:191]\n"
        "s_lshl_b32 s81, s96, 10\n"
        "buffer_load_dwordx4 v[216:219], v234, s[64:67], s81 offen sc1\n"
        "buffer_load_dwordx4 v[220:223], v235, s[64:67], s81 offen sc1\n"
        "s_add_u32 s96, s96, 1\n"
        "s_cmp_eq_u32 s96, s78\n"
        "s_cselect_b32 s96, s79, s96\n"
        "s_waitcnt vmcnt(6)\n"
        "v_mfma_f32_32x32x16_bf16 v[128:143], v[80:83], v[224:227], v[128:143]\n"
        "v_mfma_f32_32x32x16_bf16 v[144:159], v[80:83], v[228:231], v[144:159]\n"
        "v_mfma_f32_32x32x16_bf16 v[160:175], v[84:87], v[224:227], v[160:175]\n"
        "v_mfma_f32_32x32x16_bf16 v[176:191], v[84:87], v[228:231], v[176:191]\n"
        "s_lshl_b32 s81, s96, 10\n"
        "buffer_load_dwordx4 v[224:227], v234, s[64:67], s81 offen sc1\n"
        "buffer_load_dwordx4 v[228:231], v235, s[64:67], s81 offen sc1\n"
        "s_add_u32 s96, s96, 1\n"
        "s_cmp_eq_u32 s96, s78\n"
        "s_cselect_b32 s96, s79, s96\n"
        "s_waitcnt vmcnt(6)\n"
        "v_mfma_f32_32x32x16_bf16 v[128:143], v[88:91], v[200:203], v[128:143]\n"
        "v_mfma_f32_32x32x16_bf16 v[144:159], v[88:91], v[204:207], v[144:159]\n"
        "v_mfma_f32_32x32x16_bf16 v[160:175], v[92:95], v[200:203], v[160:175]\n"
        "v_mfma_f32_32x32x16_bf16 v[176:191], v[92:95], v[204:207], v[176:191]\n"
        "s_waitcnt vmcnt(4)\n"
        "v_mfma_f32_32x32x16_bf16 v[128:143], v[96:99], v[208:211], v[128:143]\n"
        "v_mfma_f32_32x32x16_bf16 v[144:159], v[96:99], v[212:215], v[144:159]\n"
        "v_mfma_f32_32x32x16_bf16 v[160:175], v[100:103], v[208:211], v[160:175]\n"
        "v_mfma_f32_32x32x16_bf16 v[176:191], v[100:103], v[212:215], v[176:191]\n"
        "s_waitcnt vmcnt(2)\n"
        "v_mfma_f32_32x32x16_bf16 v[128:143], v[104:107], v[216:219], v[128:143]\n"
        "v_mfma_f32_32x32x16_bf16 v[144:159], v[104:107], v[220:223], v[144:159]\n"
        "v_mfma_f32_32x32x16_bf16 v[160:175], v[108:111], v[216:219], v[160:175]\n"
        "v_mfma_f32_32x32x16_bf16 v[176:191], v[108:111], v[220:223], v[176:191]\n"
        "s_waitcnt vmcnt(0)\n"
        "v_mfma_f32_32x32x16_bf16 v[128:143], v[112:115], v[224:227], v[128:143]\n"
        "v_mfma_f32_32x32x16_bf16 v[144:159], v[112:115], v[228:231], v[144:159]\n"
        "v_mfma_f32_32x32x16_bf16 v[160:175], v[116:119], v[224:227], v[160:175]\n"
        "v_mfma_f32_32x32x16_bf16 v[176:191], v[116:119], v[228:231], v[176:191]\n"
        "s_setprio 1\n"
        "v_add_u32 v1, s76, v245\n"
        "v_add_u32 v7, 0x10000, v1\n"
        "s_waitcnt vmcnt(0)\n"
        "s_nop 9\n"
        "ds_write_b128 v1, v[144:147] offset:49152\n"
        "ds_write_b128 v1, v[148:151] offset:50176\n"
        "ds_write_b128 v1, v[152:155] offset:57344\n"
        "ds_write_b128 v1, v[156:159] offset:58368\n"
        "ds_write_b128 v7, v[160:163] offset:8192\n"
        "ds_write_b128 v7, v[164:167] offset:9216\n"
        "ds_write_b128 v7, v[168:171] offset:16384\n"
        "ds_write_b128 v7, v[172:175] offset:17408\n"
        "ds_write_b128 v7, v[176:179] offset:0\n"
        "ds_write_b128 v7, v[180:183] offset:1024\n"
        "ds_write_b128 v7, v[184:187] offset:24576\n"
        "ds_write_b128 v7, v[188:191] offset:25600\n"
        "s_add_u32 s14, s14, 4\n"
        "s_mov_b64 exec, 1\n"
        "ds_add_u32 v246, v247\n"
        "s_mov_b64 exec, -1\n"
        "s_mov_b32 s26, 0\n"
        ".Lspin_52:\n"
        "ds_read_b32 v1, v246\n"
        "s_waitcnt lgkmcnt(0)\n"
        ".Lsmid_54:\n"
        "v_readfirstlane_b32 s27, v1\n"
        "s_add_u32 s26, s26, 1\n"
        "s_and_b32 s25, s27, 0xffff\n"
        "s_cmp_ge_u32 s25, s14\n"
        "s_cbranch_scc1 .Lsdone_53\n"
        "s_cmp_lt_u32 s26, 400000\n"
        "s_cbranch_scc1 .Lspin_52\n"
        ".Lsdone_53:\n"
        "s_xor_b32 s18, s10, 1\n"
        "s_lshl_b32 s18, s18, 11\n"
        "v_add_u32 v6, s18, v245\n"
        "v_add_u32 v7, 0x10000, v6\n"
        "ds_read_b128 v[144:147], v6 offset:49152\n"
        "ds_read_b128 v[148:151], v6 offset:50176\n"
        "ds_read_b128 v[152:155], v6 offset:57344\n"
        "ds_read_b128 v[156:159], v6 offset:58368\n"
        "s_xor_b32 s18, s10, 2\n"
        "s_lshl_b32 s18, s18, 11\n"
        "v_add_u32 v6, s18, v245\n"
        "v_add_u32 v7, 0x10000, v6\n"
        "ds_read_b128 v[160:163], v7 offset:8192\n"
        "ds_read_b128 v[164:167], v7 offset:9216\n"
        "ds_read_b128 v[168:171], v7 offset:16384\n"
        "ds_read_b128 v[172:175], v7 offset:17408\n"
        "s_xor_b32 s18, s10, 3\n"
        "s_lshl_b32 s18, s18, 11\n"
        "v_add_u32 v6, s18, v245\n"
        "v_add_u32 v7, 0x10000, v6\n"
        "ds_read_b128 v[176:179], v7 offset:0\n"
        "ds_read_b128 v[180:183], v7 offset:1024\n"
        "ds_read_b128 v[184:187], v7 offset:24576\n"
        "ds_read_b128 v[188:191], v7 offset:25600\n"
        "s_waitcnt lgkmcnt(8)\n"
        "v_pk_add_f32 v[128:129], v[128:129], v[144:145]\n"
        "v_pk_add_f32 v[130:131], v[130:131], v[146:147]\n"
        "v_pk_add_f32 v[132:133], v[132:133], v[148:149]\n"
        "v_pk_add_f32 v[134:135], v[134:135], v[150:151]\n"
        "v_pk_add_f32 v[136:137], v[136:137], v[152:153]\n"
        "v_pk_add_f32 v[138:139], v[138:139], v[154:155]\n"
        "v_pk_add_f32 v[140:141], v[140:141], v[156:157]\n"
        "v_pk_add_f32 v[142:143], v[142:143], v[158:159]\n"
        "s_waitcnt lgkmcnt(4)\n"
        "v_pk_add_f32 v[128:129], v[128:129], v[160:161]\n"
        "v_pk_add_f32 v[130:131], v[130:131], v[162:163]\n"
        "v_pk_add_f32 v[132:133], v[132:133], v[164:165]\n"
        "v_pk_add_f32 v[134:135], v[134:135], v[166:167]\n"
        "v_pk_add_f32 v[136:137], v[136:137], v[168:169]\n"
        "v_pk_add_f32 v[138:139], v[138:139], v[170:171]\n"
        "v_pk_add_f32 v[140:141], v[140:141], v[172:173]\n"
        "v_pk_add_f32 v[142:143], v[142:143], v[174:175]\n"
        "s_waitcnt lgkmcnt(0)\n"
        "v_pk_add_f32 v[128:129], v[128:129], v[176:177]\n"
        "v_pk_add_f32 v[130:131], v[130:131], v[178:179]\n"
        "v_pk_add_f32 v[132:133], v[132:133], v[180:181]\n"
        "v_pk_add_f32 v[134:135], v[134:135], v[182:183]\n"
        "v_pk_add_f32 v[136:137], v[136:137], v[184:185]\n"
        "v_pk_add_f32 v[138:139], v[138:139], v[186:187]\n"
        "v_pk_add_f32 v[140:141], v[140:141], v[188:189]\n"
        "v_pk_add_f32 v[142:143], v[142:143], v[190:191]\n"
        "s_lshr_b32 s19, s10, 1\n"
        "s_lshl_b32 s18, s19, 12\n"
        "v_add_u32 v2, s18, v236\n"
        "v_add_u32 v3, s18, v237\n"
        "v_add_u32 v4, 0x10000, v2\n"
        "v_add_u32 v5, 0x10000, v3\n"
        "ds_read_b64_tr_b16 v[16:17], v4 offset:32768\n"
        "ds_read_b64_tr_b16 v[18:19], v4 offset:33280\n"
        "ds_read_b64_tr_b16 v[20:21], v5 offset:32768\n"
        "ds_read_b64_tr_b16 v[22:23], v5 offset:33280\n"
        "ds_read_b64_tr_b16 v[24:25], v2 offset:0\n"
        "ds_read_b64_tr_b16 v[26:27], v2 offset:512\n"
        "ds_read_b64_tr_b16 v[28:29], v3 offset:0\n"
        "ds_read_b64_tr_b16 v[30:31], v3 offset:512\n"
        "ds_read_b64_tr_b16 v[32:33], v2 offset:8192\n"
        "ds_read_b64_tr_b16 v[34:35], v2 offset:8704\n"
        "ds_read_b64_tr_b16 v[36:37], v3 offset:8192\n"
        "ds_read_b64_tr_b16 v[38:39], v3 offset:8704\n"
        "s_waitcnt lgkmcnt(8)\n"
        "ds_read_b64_tr_b16 v[40:41], v2 offset:16384\n"
        "ds_read_b64_tr_b16 v[42:43], v2 offset:16896\n"
        "ds_read_b64_tr_b16 v[44:45], v3 offset:16384\n"
        "ds_read_b64_tr_b16 v[46:47], v3 offset:16896\n"
        "s_waitcnt lgkmcnt(8)\n"
        "ds_read_b64_tr_b16 v[48:49], v2 offset:24576\n"
        "ds_read_b64_tr_b16 v[50:51], v2 offset:25088\n"
        "ds_read_b64_tr_b16 v[52:53], v3 offset:24576\n"
        "ds_read_b64_tr_b16 v[54:55], v3 offset:25088\n"
        "s_waitcnt lgkmcnt(8)\n"
        "ds_read_b64_tr_b16 v[56:57], v2 offset:32768\n"
        "ds_read_b64_tr_b16 v[58:59], v2 offset:33280\n"
        "ds_read_b64_tr_b16 v[60:61], v3 offset:32768\n"
        "ds_read_b64_tr_b16 v[62:63], v3 offset:33280\n"
        "s_waitcnt lgkmcnt(8)\n"
        "ds_read_b64_tr_b16 v[64:65], v2 offset:40960\n"
        "ds_read_b64_tr_b16 v[66:67], v2 offset:41472\n"
        "ds_read_b64_tr_b16 v[68:69], v3 offset:40960\n"
        "ds_read_b64_tr_b16 v[70:71], v3 offset:41472\n"
        "s_waitcnt lgkmcnt(8)\n"
        "ds_read_b64_tr_b16 v[72:73], v4 offset:34816\n"
        "ds_read_b64_tr_b16 v[74:75], v4 offset:35328\n"
        "ds_read_b64_tr_b16 v[76:77], v5 offset:34816\n"
        "ds_read_b64_tr_b16 v[78:79], v5 offset:35328\n"
        "s_waitcnt lgkmcnt(8)\n"
        "ds_read_b64_tr_b16 v[80:81], v2 offset:2048\n"
        "ds_read_b64_tr_b16 v[82:83], v2 offset:2560\n"
        "ds_read_b64_tr_b16 v[84:85], v3 offset:2048\n"
        "ds_read_b64_tr_b16 v[86:87], v3 offset:2560\n"
        "s_waitcnt lgkmcnt(8)\n"
        "ds_read_b64_tr_b16 v[88:89], v2 offset:10240\n"
        "ds_read_b64_tr_b16 v[90:91], v2 offset:10752\n"
        "ds_read_b64_tr_b16 v[92:93], v3 offset:10240\n"
        "ds_read_b64_tr_b16 v[94:95], v3 offset:10752\n"
        "s_waitcnt lgkmcnt(8)\n"
        "ds_read_b64_tr_b16 v[96:97], v2 offset:18432\n"
        "ds_read_b64_tr_b16 v[98:99], v2 offset:18944\n"
        "ds_read_b64_tr_b16 v[100:101], v3 offset:18432\n"
        "ds_read_b64_tr_b16 v[102:103], v3 offset:18944\n"
        "s_waitcnt lgkmcnt(8)\n"
        "ds_read_b64_tr_b16 v[104:105], v2 offset:26624\n"
        "ds_read_b64_tr_b16 v[106:107], v2 offset:27136\n"
        "ds_read_b64_tr_b16 v[108:109], v3 offset:26624\n"
        "ds_read_b64_tr_b16 v[110:111], v3 offset:27136\n"
        "s_waitcnt lgkmcnt(8)\n"
        "ds_read_b64_tr_b16 v[112:113], v2 offset:34816\n"
        "ds_read_b64_tr_b16 v[114:115], v2 offset:35328\n"
        "ds_read_b64_tr_b16 v[116:117], v3 offset:34816\n"
        "ds_read_b64_tr_b16 v[118:119], v3 offset:35328\n"
        "s_waitcnt lgkmcnt(8)\n"
        "ds_read_b64_tr_b16 v[120:121], v2 offset:43008\n"
        "ds_read_b64_tr_b16 v[122:123], v2 offset:43520\n"
        "ds_read_b64_tr_b16 v[124:125], v3 offset:43008\n"
        "ds_read_b64_tr_b16 v[126:127], v3 offset:43520\n"
        "v_and_b32 v1, 7, v0\n"
        "s_lshl_b32 s18, s19, 3\n"
        "v_add_u32 v1, s18, v1\n"
        "s_lshr_b32 s18, s75, 1\n"
        "v_lshl_add_u32 v1, v1, 4, s18\n"
        "v_lshlrev_b32 v1, 7, v1\n"
        "v_add_u32 v4, 69632, v1\n"
        "s_and_b32 s18, s10, 1\n"
        "s_lshl_b32 s20, s75, 1\n"
        "s_add_u32 s18, s18, s20\n"
        "s_lshl_b32 s18, s18, 1\n"
        "s_add_u32 s18, s18, s19\n"
        "s_mul_i32 s71, s18, 14336\n"
        "s_add_u32 s71, s71, 4194304\n"
        "s_waitcnt lgkmcnt(0)\n"
        "s_add_u32 s14, s14, 4\n"
        "s_mov_b64 exec, 1\n"
        "ds_add_u32 v246, v247\n"
        "s_mov_b64 exec, -1\n"
        "s_mov_b32 s26, 0\n"
        ".Lspin_55:\n"
        "ds_read_b32 v1, v246\n"
        "s_waitcnt lgkmcnt(0)\n"
        ".Lsmid_57:\n"
        "v_readfirstlane_b32 s27, v1\n"
        "s_add_u32 s26, s26, 1\n"
        "s_and_b32 s25, s27, 0xffff\n"
        "s_cmp_ge_u32 s25, s14\n"
        "s_cbranch_scc1 .Lsdone_56\n"
        "s_cmp_lt_u32 s26, 400000\n"
        "s_cbranch_scc1 .Lspin_55\n"
        ".Lsdone_56:\n"
        "s_setprio 0\n"
        "s_mov_b32 s26, 0\n"
        ".Lpoll_58:\n"
        "buffer_load_dwordx2 v[6:7], v4, s[64:67], 0 offen sc1\n"
        "s_add_u32 s26, s26, 1\n"
        "s_waitcnt vmcnt(0)\n"
        "s_setprio 1\n"
        "v_cmp_ne_u64 vcc, s[68:69], v[6:7]\n"
        "s_setprio 0\n"
        "s_cbranch_vccz .Lpdone_59\n"
        "s_sleep 4\n"
        "s_cmp_lt_u32 s26, 50000\n"
        "s_cbranch_scc1 .Lpoll_58\n"
        ".Lpdone_59:\n"
        "s_setprio 3\n"
        "buffer_load_dwordx4 v[160:163], v245, s[64:67], s71 offen offset:0 sc1\n"
        "buffer_load_dwordx4 v[164:167], v245, s[64:67], s71 offen offset:1024 sc1\n"
        "buffer_load_dwordx4 v[168:171], v245, s[64:67], s71 offen offset:2048 sc1\n"
        "buffer_load_dwordx4 v[172:175], v245, s[64:67], s71 offen offset:3072 sc1\n"
        "s_add_u32 s71, s71, 4096\n"
        "buffer_load_dwordx4 v[176:179], v245, s[64:67], s71 offen offset:0 sc1\n"
        "buffer_load_dwordx4 v[180:183], v245, s[64:67], s71 offen offset:1024 sc1\n"
        "buffer_load_dwordx4 v[184:187], v245, s[64:67], s71 offen offset:2048 sc1\n"
        "buffer_load_dwordx4 v[188:191], v245, s[64:67], s71 offen offset:3072 sc1\n"
        "s_add_u32 s71, s71, 4096\n"
        "buffer_load_dwordx4 v[192:195], v245, s[64:67], s71 offen offset:0 sc1\n"
        "buffer_load_dwordx4 v[196:199], v245, s[64:67], s71 offen offset:1024 sc1\n"
        "buffer_load_dwordx4 v[200:203], v245, s[64:67], s71 offen offset:2048 sc1\n"
        "buffer_load_dwordx4 v[204:207], v245, s[64:67], s71 offen offset:3072 sc1\n"
        "s_add_u32 s71, s71, 4096\n"
        "buffer_load_dwordx4 v[208:211], v245, s[64:67], s71 offen offset:0 sc1\n"
        "buffer_load_dwordx4 v[212:215], v245, s[64:67], s71 offen offset:1024 sc1\n"
        "s_lshl_b32 s18, s10, 12\n"
        "v_add_u32 v1, s18, v245\n"
        "s_xor_b32 s18, s18, 0x2000\n"
        "v_add_u32 v2, s18, v245\n"
        "s_waitcnt vmcnt(13)\n"
        "v_mfma_f32_32x32x16_bf16 v[144:159], v[20:23], v[160:163], 0\n"
        "v_mfma_f32_32x32x16_bf16 v[128:143], v[16:19], v[160:163], v[128:143]\n"
        "s_waitcnt vmcnt(12)\n"
        "v_mfma_f32_32x32x16_bf16 v[144:159], v[28:31], v[164:167], v[144:159]\n"
        "v_mfma_f32_32x32x16_bf16 v[128:143], v[24:27], v[164:167], v[128:143]\n"
        "s_waitcnt vmcnt(11)\n"
        "v_mfma_f32_32x32x16_bf16 v[144:159], v[36:39], v[168:171], v[144:159]\n"
        "v_mfma_f32_32x32x16_bf16 v[128:143], v[32:35], v[168:171], v[128:143]\n"
        "s_waitcnt vmcnt(10)\n"
        "v_mfma_f32_32x32x16_bf16 v[144:159], v[44:47], v[172:175], v[144:159]\n"
        "v_mfma_f32_32x32x16_bf16 v[128:143], v[40:43], v[172:175], v[128:143]\n"
        "s_waitcnt vmcnt(9)\n"
        "v_mfma_f32_32x32x16_bf16 v[144:159], v[52:55], v[176:179], v[144:159]\n"
        "v_mfma_f32_32x32x16_bf16 v[128:143], v[48:51], v[176:179], v[128:143]\n"
        "s_waitcnt vmcnt(8)\n"
        "v_mfma_f32_32x32x16_bf16 v[144:159], v[60:63], v[180:183], v[144:159]\n"
        "v_mfma_f32_32x32x16_bf16 v[128:143], v[56:59], v[180:183], v[128:143]\n"
        "s_waitcnt vmcnt(7)\n"
        "v_mfma_f32_32x32x16_bf16 v[144:159], v[68:71], v[184:187], v[144:159]\n"
        "v_mfma_f32_32x32x16_bf16 v[128:143], v[64:67], v[184:187], v[128:143]\n"
        "s_waitcnt vmcnt(6)\n"
        "v_mfma_f32_32x32x16_bf16 v[144:159], v[76:79], v[188:191], v[144:159]\n"
        "v_mfma_f32_32x32x16_bf16 v[128:143], v[72:75], v[188:191], v[128:143]\n"
        "s_waitcnt vmcnt(5)\n"
        "v_mfma_f32_32x32x16_bf16 v[144:159], v[84:87], v[192:195], v[144:159]\n"
        "v_mfma_f32_32x32x16_bf16 v[128:143], v[80:83], v[192:195], v[128:143]\n"
        "s_waitcnt vmcnt(4)\n"
        "v_mfma_f32_32x32x16_bf16 v[144:159], v[92:95], v[196:199], v[144:159]\n"
        "v_mfma_f32_32x32x16_bf16 v[128:143], v[88:91], v[196:199], v[128:143]\n"
        "s_waitcnt vmcnt(3)\n"
        "v_mfma_f32_32x32x16_bf16 v[144:159], v[100:103], v[200:203], v[144:159]\n"
        "v_mfma_f32_32x32x16_bf16 v[128:143], v[96:99], v[200:203], v[128:143]\n"
        "s_waitcnt vmcnt(2)\n"
        "v_mfma_f32_32x32x16_bf16 v[144:159], v[108:111], v[204:207], v[144:159]\n"
        "v_mfma_f32_32x32x16_bf16 v[128:143], v[104:107], v[204:207], v[128:143]\n"
        "s_waitcnt vmcnt(1)\n"
        "v_mfma_f32_32x32x16_bf16 v[144:159], v[116:119], v[208:211], v[144:159]\n"
        "v_mfma_f32_32x32x16_bf16 v[128:143], v[112:115], v[208:211], v[128:143]\n"
        "s_waitcnt vmcnt(0)\n"
        "v_mfma_f32_32x32x16_bf16 v[144:159], v[124:127], v[212:215], v[144:159]\n"
        "v_mfma_f32_32x32x16_bf16 v[128:143], v[120:123], v[212:215], v[128:143]\n"
        "s_nop 10\n"
        "ds_write_b128 v1, v[144:147] offset:0\n"
        "ds_write_b128 v1, v[148:151] offset:1024\n"
        "ds_write_b128 v1, v[152:155] offset:2048\n"
        "ds_write_b128 v1, v[156:159] offset:3072\n"
        "s_add_u32 s14, s14, 4\n"
        "s_mov_b64 exec, 1\n"
        "ds_add_u32 v246, v247\n"
        "s_mov_b64 exec, -1\n"
        "s_mov_b32 s26, 0\n"
        ".Lspin_60:\n"
        "ds_read_b32 v3, v246\n"
        "s_waitcnt lgkmcnt(0)\n"
        ".Lsmid_62:\n"
        "v_readfirstlane_b32 s27, v3\n"
        "s_add_u32 s26, s26, 1\n"
        "s_and_b32 s25, s27, 0xffff\n"
        "s_cmp_ge_u32 s25, s14\n"
        "s_cbranch_scc1 .Lsdone_61\n"
        "s_cmp_lt_u32 s26, 400000\n"
        "s_cbranch_scc1 .Lspin_60\n"
        ".Lsdone_61:\n"
        "ds_read_b128 v[160:163], v2 offset:0\n"
        "ds_read_b128 v[164:167], v2 offset:1024\n"
        "ds_read_b128 v[168:171], v2 offset:2048\n"
        "ds_read_b128 v[172:175], v2 offset:3072\n"
        "s_waitcnt lgkmcnt(2)\n"
        "v_pk_add_f32 v[128:129], v[128:129], v[160:161]\n"
        "v_pk_add_f32 v[130:131], v[130:131], v[162:163]\n"
        "v_pk_add_f32 v[132:133], v[132:133], v[164:165]\n"
        "v_pk_add_f32 v[134:135], v[134:135], v[166:167]\n"
        "v_cvt_pk_bf16_f32 v16, v128, v129\n"
        "v_cvt_pk_bf16_f32 v17, v130, v131\n"
        "v_cvt_pk_bf16_f32 v18, v132, v133\n"
        "v_cvt_pk_bf16_f32 v19, v134, v135\n"
        "buffer_store_dwordx4 v[16:19], v238, s[60:63], 0 offen offset:0\n"
        "s_waitcnt lgkmcnt(0)\n"
        "v_pk_add_f32 v[136:137], v[136:137], v[168:169]\n"
        "v_pk_add_f32 v[138:139], v[138:139], v[170:171]\n"
        "v_pk_add_f32 v[140:141], v[140:141], v[172:173]\n"
        "v_pk_add_f32 v[142:143], v[142:143], v[174:175]\n"
        "v_cvt_pk_bf16_f32 v20, v136, v137\n"
        "v_cvt_pk_bf16_f32 v21, v138, v139\n"
        "v_cvt_pk_bf16_f32 v22, v140, v141\n"
        "v_cvt_pk_bf16_f32 v23, v142, v143\n"
        "buffer_store_dwordx4 v[20:23], v238, s[60:63], 0 offen offset:32\n"
        "s_endpgm\n"
        ".Lcode_end:\n"
        :
        : "s"((unsigned long long)__builtin_amdgcn_kernarg_segment_ptr()), "s"(blockIdx.x), "v"(threadIdx.x),
          "s"((unsigned)(unsigned long long)lds)
        : "memory", "vcc", "scc", "s8", "s9", "s10", "s11", "s12", "s13", "s14", "s15", "s16", "s17", "s18", "s19", "s20", "s21", "s22", "s23", "s24", "s25", "s26", "s27", "s28", "s29", "s30", "s31", "s32", "s33", "s34", "s35", "s36", "s37", "s38", "s39", "s40", "s41", "s42", "s43", "s44", "s45", "s46", "s47", "s48", "s49", "s50", "s51", "s52", "s53", "s54", "s55", "s56", "s57", "s58", "s59", "s60", "s61", "s62", "s63", "s64", "s65", "s66", "s67", "s68", "s69", "s70", "s71", "s72", "s73", "s74", "s75", "s76", "s77", "s78", "s79", "s80", "s81", "s82", "s83", "s84", "s85", "s86", "s87", "s88", "s89", "s90", "s91", "s92", "s93", "s94", "s95", "s96", "s97", "s98", "s99", "s100", "s101",
          "v1", "v2", "v3", "v4", "v5", "v6", "v7", "v8", "v9", "v10", "v11", "v12", "v13", "v14", "v15", "v16", "v17", "v18", "v19", "v20", "v21", "v22", "v23", "v24", "v25", "v26", "v27", "v28", "v29", "v30", "v31", "v32", "v33", "v34", "v35", "v36", "v37", "v38", "v39", "v40", "v41", "v42", "v43", "v44", "v45", "v46", "v47", "v48", "v49", "v50", "v51", "v52", "v53", "v54", "v55", "v56", "v57", "v58", "v59", "v60", "v61", "v62", "v63", "v64", "v65", "v66", "v67", "v68", "v69", "v70", "v71", "v72", "v73", "v74", "v75", "v76", "v77", "v78", "v79", "v80", "v81", "v82", "v83", "v84", "v85", "v86", "v87", "v88", "v89", "v90", "v91", "v92", "v93", "v94", "v95", "v96", "v97", "v98", "v99", "v100", "v101", "v102", "v103", "v104", "v105", "v106", "v107", "v108", "v109", "v110", "v111", "v112", "v113", "v114", "v115", "v116", "v117", "v118", "v119", "v120", "v121", "v122", "v123", "v124", "v125", "v126", "v127", "v128", "v129", "v130", "v131", "v132", "v133", "v134", "v135", "v136", "v137", "v138", "v139", "v140", "v141", "v142", "v143", "v144", "v145", "v146", "v147", "v148", "v149", "v150", "v151", "v152", "v153", "v154", "v155", "v156", "v157", "v158", "v159", "v160", "v161", "v162", "v163", "v164", "v165", "v166", "v167", "v168", "v169", "v170", "v171", "v172", "v173", "v174", "v175", "v176", "v177", "v178", "v179", "v180", "v181", "v182", "v183", "v184", "v185", "v186", "v187", "v188", "v189", "v190", "v191", "v192", "v193", "v194", "v195", "v196", "v197", "v198", "v199", "v200", "v201", "v202", "v203", "v204", "v205", "v206", "v207", "v208", "v209", "v210", "v211", "v212", "v213", "v214", "v215", "v216", "v217", "v218", "v219", "v220", "v221", "v222", "v223", "v224", "v225", "v226", "v227", "v228", "v229", "v230", "v231", "v232", "v233", "v234", "v235", "v236", "v237", "v238", "v239", "v240", "v241", "v242", "v243", "v244", "v245", "v246", "v247", "v248", "v249", "v250", "v251", "v252", "v253", "v254", "v255");
}

extern "C" void kernel_launch(void* const* d_in, const int* in_sizes, int n_in,
                              void* d_out, int out_size, void* d_ws, size_t ws_size,
                              hipStream_t stream) {
    Args a;
    const int order[7] = {0, 1, 2, 4, 5, 6, 7};
    for (int i = 0; i < 7; i++) { a.s[i].p = d_in[order[i]]; a.s[i].bytes = (unsigned)in_sizes[order[i]] * 2u; a.s[i].flags = 0x20000u; }
    a.s[7].p = d_out; a.s[7].bytes = (unsigned)out_size * 2u; a.s[7].flags = 0x20000u;
    a.s[8].p = d_ws;  a.s[8].bytes = 0xe00000u;                a.s[8].flags = 0x20000u;
    fused_block_kernel<<<256, 512, 0, stream>>>(a);
}
```
